```python
import torch

def _softmax(x, axis=-1):
    return torch.softmax(x, dim=axis)
def _gelu(x):
    return torch.nn.functional.gelu(x, approximate="tanh")
def _silu(x):
    return torch.nn.functional.silu(x)
def _sigmoid(x):
    return torch.sigmoid(x)
def _layernorm(x, axis=-1, eps=1e-5):
    m = x.mean(dim=axis, keepdim=True)
    v = x.var(dim=axis, keepdim=True, unbiased=False)
    return (x - m) / torch.sqrt(v + eps)
def _scatter_add(tgt, idx, src, axis=0):
    return tgt.clone().index_add_(axis, idx, src)
def _segment_sum(src, seg, n):
    out = torch.zeros((n,) + src.shape[1:], dtype=src.dtype)
    return out.index_add_(0, seg, src)
def _pad(x, pw):
    flat = tuple(v for pair in reversed(pw) for v in pair)
    return torch.nn.functional.pad(x, flat)

kernel_name = 'interleave_layers__spectral_gfnet__glu_gated_reglu__prompt'

def setup_inputs():

    _w = (lambda a: (lambda t: t.to(torch.bfloat16) if t.is_floating_point() else t)(torch.as_tensor(a)))
    def _A_setup():
        B, S, D = 1, 2048, 512
        Df = D // 2 + 1
        return {
            "x": _w((lambda *s: torch.randn(*s, dtype=torch.bfloat16) * 0.05)(B, S, D)),
            "w_r": _w((lambda *s: torch.randn(*s, dtype=torch.bfloat16) * 0.4)(S, Df) * 0.02),
            "w_i": _w((lambda *s: torch.randn(*s, dtype=torch.bfloat16) * 0.4)(S, Df) * 0.02),
        }
    _A_in = _A_setup()

    def _B_setup():
        B, S, D, F = 1, 2048, 512, 1376
        return {
            "x": _w((lambda *s: torch.randn(*s, dtype=torch.bfloat16) * 0.05)(B, S, D)),
            "W_gate": _w((lambda *s: torch.randn(*s, dtype=torch.bfloat16) * 0.2)(D, F) * (2.0 / D) ** 0.5),
            "W_up": _w((lambda *s: torch.randn(*s, dtype=torch.bfloat16) * 0.2)(D, F) * (2.0 / D) ** 0.5),
            "W_down": _w((lambda *s: torch.randn(*s, dtype=torch.bfloat16) * 0.2)(F, D) * (1.0 / F) ** 0.5),
        }
    _B_in = _B_setup()

    return {
        "A_x": _A_in["x"],
        "A_w_r": _A_in["w_r"],
        "A_w_i": _A_in["w_i"],
        "B_x": _B_in["x"],
        "B_W_gate": _B_in["W_gate"],
        "B_W_up": _B_in["W_up"],
        "B_W_down": _B_in["W_down"],
    }

def reference(A_x, A_w_r, A_w_i, B_x, B_W_gate, B_W_up, B_W_down):
    def _A_ref(x, w_r, w_i):
        B, S, D = x.shape
        X = (lambda x, axes=(-2,-1): torch.fft.rfft2(x.float(), dim=axes))(x, axes=(-2, -1))
        W = w_r + 1j * w_i
        X = X * W
        y = (lambda x, s, axes=(-2,-1): torch.fft.irfft2(x, s=s, dim=axes).to(torch.bfloat16))(X, (S, D), axes=(-2, -1))
        return y

    def _B_ref(x, W_gate, W_up, W_down):
        gate = torch.einsum("bsd,df->bsf", x, W_gate)
        up = torch.einsum("bsd,df->bsf", x, W_up)
        gate = gate * (lambda x, dt: x.to(dt))(gate > 0, gate.dtype)
        h = gate * up
        return torch.einsum("bsf,fd->bsd", h, W_down)

    x = A_x
    x = x + _A_ref(_layernorm(x, axis=-1), A_w_r, A_w_i)
    x = x + _B_ref(_layernorm(x, axis=-1), B_W_gate, B_W_up, B_W_down)
    x = x + _A_ref(_layernorm(x, axis=-1), A_w_r, A_w_i)
    x = x + _B_ref(_layernorm(x, axis=-1), B_W_gate, B_W_up, B_W_down)
    return x
```

```cpp
#include <hip/hip_runtime.h>
#include <hip/hip_bf16.h>

typedef unsigned short u16;
typedef unsigned int u32;
typedef float f32x2 __attribute__((ext_vector_type(2)));
typedef float f32x4 __attribute__((ext_vector_type(4)));
typedef float f32x16 __attribute__((ext_vector_type(16)));
typedef u32 u32x2 __attribute__((ext_vector_type(2)));
typedef u32 u32x4 __attribute__((ext_vector_type(4)));
typedef __bf16 bf2 __attribute__((ext_vector_type(2)));
typedef __bf16 bf8 __attribute__((ext_vector_type(8)));
typedef short s16x4 __attribute__((ext_vector_type(4)));

#define DEV __device__ __forceinline__
constexpr int NF = 86;
constexpr u32 SGN_HI = 0x80000000u, SGN_LO = 0x00008000u, SGN_BOTH = 0x80008000u;

DEV u32 pk(float a, float b) { f32x2 v = {a, b}; return __builtin_bit_cast(u32, __builtin_convertvector(v, bf2)); }
DEV float blo(u32 v) { return __builtin_bit_cast(float, v << 16); }
DEV float bhi(u32 v) { return __builtin_bit_cast(float, v & 0xffff0000u); }
DEV float cosr(float r) { return __builtin_amdgcn_cosf(r); }
DEV float sinr(float r) { return __builtin_amdgcn_sinf(r); }
DEV f32x4 mma32(u32x4 a, u32x4 b, f32x4 c) { return __builtin_amdgcn_mfma_f32_16x16x32_bf16(__builtin_bit_cast(bf8, a), __builtin_bit_cast(bf8, b), c, 0, 0, 0); }
DEV f32x4 mma16(u32x2 a, u32x2 b, f32x4 c) { return __builtin_amdgcn_mfma_f32_16x16x16bf16_1k(__builtin_bit_cast(s16x4, a), __builtin_bit_cast(s16x4, b), c, 0, 0, 0); }
DEV f32x16 mmb(u32x4 a, u32x4 b, f32x16 c) { return __builtin_amdgcn_mfma_f32_32x32x16_bf16(__builtin_bit_cast(bf8, a), __builtin_bit_cast(bf8, b), c, 0, 0, 0); }
template <int CTRL> DEV float dpp(float v) { return __builtin_bit_cast(float, __builtin_amdgcn_update_dpp(0, __builtin_bit_cast(int, v), CTRL, 0xf, 0xf, true)); }
DEV float wave_sum(float v) {
  v += dpp<0xB1>(v); v += dpp<0x4E>(v); v += dpp<0x141>(v); v += dpp<0x140>(v);
  u32 u = __builtin_bit_cast(u32, v);
  auto r = __builtin_amdgcn_permlane16_swap(u, u, false, false);
  const u32 r0 = r[0], r1 = r[1];
  v = __builtin_bit_cast(float, r0) + __builtin_bit_cast(float, r1);
  u = __builtin_bit_cast(u32, v);
  auto q = __builtin_amdgcn_permlane32_swap(u, u, false, false);
  const u32 q0 = q[0], q1 = q[1];
  return __builtin_bit_cast(float, q0) + __builtin_bit_cast(float, q1);
}
DEV u32x4 packc(const f32x4& re, const f32x4& im) { return u32x4{pk(re[0], re[1]), pk(re[2], re[3]), pk(im[0], im[1]), pk(im[2], im[3])}; }
const f32x4 __device__ Z4 = {0.f, 0.f, 0.f, 0.f};

DEV void pre_filter(int pb, const u16* wr, const u16* wi, u32x4* WT) {
  const int tid = threadIdx.x, kb = pb & 7, r = pb >> 3, m2 = r >> 3, g = (r >> 1) & 3, c = 8 * (r & 1) + (tid & 7);
  const int k = 32 * kb + (tid >> 3), kk = k ? k : 256;
  const float sc = k ? 0x1p-19f : 0x1p-20f;
  u32x4 o;
#pragma unroll
  for (int i = 0; i < 4; ++i) {
    const int m = m2 + 8 * (c + 16 * (4 * g + i));
    o[i] = pk(blo(wr[m * 257 + kk]) * sc, blo(wi[m * 257 + kk]) * sc);
  }
  WT[(k * 8 + m2) * 64 + 16 * g + c] = o;
}
DEV void pre_gu(int pb, const u16* wg, const u16* wu, u32x4* WG) {
  const int l = threadIdx.x & 63, F = (pb >> 5) * 4 + (threadIdx.x >> 6), ks = pb & 31;
  if (F >= NF) return;
  const int r = l & 31, lam = 2 * ks + (l >> 5), d0 = 16 * (lam & 15) + 4 * (lam >> 4);
  const u16* src = (r < 16 ? wg : wu) + 16 * F + (r & 15);
  u32 v[8];
#pragma unroll
  for (int j = 0; j < 8; ++j) v[j] = src[(d0 + (j & 3) + 256 * (j >> 2)) * 1376];
  WG[(F * 32 + ks) * 64 + l] = u32x4{v[0] | v[1] << 16, v[2] | v[3] << 16, v[4] | v[5] << 16, v[6] | v[7] << 16};
}
DEV void pre_dn(int pb, const u16* wd, u32x4* WD) {
  const int l = threadIdx.x & 63, nt = (pb & 3) * 4 + (threadIdx.x >> 6), F = pb >> 2;
  const int r = l & 31, h = l >> 5, n = 32 * nt + 16 * ((r >> 2) & 1) + (r & 3) + 4 * (r >> 3);
  u32 v[8];
#pragma unroll
  for (int j = 0; j < 8; ++j) v[j] = wd[(16 * F + (j & 3) + 8 * (j >> 2) + 4 * h) * 512 + n];
  WD[(nt * NF + F) * 64 + l] = u32x4{v[0] | v[1] << 16, v[2] | v[3] << 16, v[4] | v[5] << 16, v[6] | v[7] << 16};
}

__global__ __launch_bounds__(256) void k_a1(const u16* __restrict__ x, u32x2* __restrict__ Z,
                                            const u16* __restrict__ wr, const u16* __restrict__ wi, u32x4* __restrict__ WT) {
  __shared__ u32x2 L[4][264];
  if (blockIdx.x >= 256) { pre_filter(blockIdx.x - 256, wr, wi, WT); return; }
  const int s1 = (blockIdx.x & 7) * 32 + (blockIdx.x >> 3);
  const int w = threadIdx.x >> 6, l = threadIdx.x & 63, c = l & 15, g = l >> 4;
  float xv[2][8];
#pragma unroll
  for (int e = 0; e < 2; ++e)
#pragma unroll
    for (int j = 0; j < 8; ++j) xv[e][j] = blo(x[(s1 + 256 * (2 * w + e)) * 512 + 128 * g + 16 * j + c]);
  u32x4 Bc[2], Bs[2], Am; u32x2 G; float tc[2][4], ts[2][4];
#pragma unroll
  for (int p = 0; p < 4; ++p) {
    const float a0 = (float)(((8 * g + 2 * p) * c) & 31) * (1.f / 32), a1 = (float)(((8 * g + 2 * p + 1) * c) & 31) * (1.f / 32);
    Bc[0][p] = pk(cosr(a0), cosr(a1)); Bs[0][p] = pk(-sinr(a0), -sinr(a1));
    Bc[1][p] = Bc[0][p] ^ SGN_HI; Bs[1][p] = Bs[0][p] ^ SGN_HI;
  }
#pragma unroll
  for (int t = 0; t < 2; ++t)
#pragma unroll
    for (int i = 0; i < 4; ++i) { const float a = (float)((c + 16 * t) * (4 * g + i)) * (1.f / 512); tc[t][i] = cosr(a); ts[t][i] = sinr(a); }
  {
    float cs[4], sn[4];
#pragma unroll
    for (int j = 0; j < 4; ++j) { const float a = (float)(((4 * g + j) * (c >> 1)) & 15) * (1.f / 16); cs[j] = cosr(a); sn[j] = sinr(a); }
    const u32 c0 = pk(cs[0], cs[1]), c1 = pk(cs[2], cs[3]), s0 = pk(sn[0], sn[1]), s1_ = pk(sn[2], sn[3]);
    Am = (c & 1) ? u32x4{s0 ^ SGN_BOTH, s1_ ^ SGN_BOTH, c0, c1} : u32x4{c0, c1, s0, s1_};
#pragma unroll
    for (int q = 0; q < 2; ++q) {
      const float a = (float)(((c >> 1) * (s1 + 256 * (2 * g + q))) & 2047) * (1.f / 2048), cc = cosr(a), ss = sinr(a);
      G[q] = (c & 1) ? pk(-ss, cc) : pk(cc, ss);
    }
  }
  const u32 ny = c == 0 ? 0xBF803F80u : 0u;
  const u32x4 An = {ny, ny, 0u, 0u};
  f32x4 R[2][2];
#pragma unroll
  for (int e = 0; e < 2; ++e) {
    float s = 0.f;
#pragma unroll
    for (int j = 0; j < 8; ++j) s += xv[e][j];
    const float mean = wave_sum(s) * (1.f / 512);
    float q = 0.f;
#pragma unroll
    for (int j = 0; j < 8; ++j) { xv[e][j] -= mean; q += xv[e][j] * xv[e][j]; }
    const float rs = __builtin_amdgcn_rsqf(wave_sum(q) * (1.f / 512) + 1e-5f);
    const u32x4 a = {pk(xv[e][0] * rs, xv[e][1] * rs), pk(xv[e][2] * rs, xv[e][3] * rs), pk(xv[e][4] * rs, xv[e][5] * rs), pk(xv[e][6] * rs, xv[e][7] * rs)};
#pragma unroll
    for (int t = 0; t < 2; ++t) {
      const f32x4 dr = mma32(a, Bc[t], Z4), di = mma32(a, Bs[t], Z4);
      f32x4 re, im;
#pragma unroll
      for (int i = 0; i < 4; ++i) { re[i] = dr[i] * tc[t][i] + di[i] * ts[t][i]; im[i] = di[i] * tc[t][i] - dr[i] * ts[t][i]; }
      const u32x4 b = packc(re, im);
      R[e][t] = mma32(Am, b, Z4);
      if (t == 0) { const f32x4 n = mma32(An, b, Z4); if (l == 0) { R[e][0][0] = n[0]; R[e][0][1] = 0.f; } }
    }
  }
#pragma unroll
  for (int t = 0; t < 2; ++t)
#pragma unroll
    for (int q = 0; q < 2; ++q)
      L[w][c + 16 * t + 32 * (2 * g + q)] = u32x2{pk(R[0][t][2 * q], R[0][t][2 * q + 1]), pk(R[1][t][2 * q], R[1][t][2 * q + 1])};
  __syncthreads();
#pragma unroll
  for (int u = 0; u < 4; ++u) {
    const int k = c + 16 * (4 * w + u);
    const f32x4 o = mma16(G, L[g][k], Z4);
    Z[(k * 256 + s1) * 4 + g] = u32x2{pk(o[0], o[1]), pk(o[2], o[3])};
  }
}

__global__ __launch_bounds__(256) void k_a2(const u32x2* __restrict__ Z, const u32x4* __restrict__ WT, u32x4* __restrict__ Y,
                                            const u16* __restrict__ wg, const u16* __restrict__ wu, u32x4* __restrict__ WG) {
  __shared__ __attribute__((aligned(16))) u32x2 L[256 * 4];
  if (blockIdx.x >= 256) { pre_gu(blockIdx.x - 256, wg, wu, WG); return; }
  const int k = (blockIdx.x & 7) * 32 + (blockIdx.x >> 3);
  const int w = threadIdx.x >> 6, l = threadIdx.x & 63, c = l & 15, g = l >> 4;
  u32x2 u[4]; u32x4 wt[2];
#pragma unroll
  for (int j = 0; j < 4; ++j) u[j] = Z[(k * 256 + 16 * (4 * g + j) + c) * 4 + w];
#pragma unroll
  for (int e = 0; e < 2; ++e) wt[e] = WT[(k * 8 + 2 * w + e) * 64 + l];
  float cs[4], sn[4], tc[4], ts[4];
#pragma unroll
  for (int j = 0; j < 4; ++j) {
    const int p = (4 * g + j) * c;
    const float a = (float)(p & 15) * (1.f / 16), b = (float)p * (1.f / 256);
    cs[j] = cosr(a); sn[j] = sinr(a); tc[j] = cosr(b); ts[j] = sinr(b);
  }
  const u32x4 B1r = {pk(cs[0], sn[0]), pk(cs[1], sn[1]), pk(cs[2], sn[2]), pk(cs[3], sn[3])};
  const u32x4 B1i = {pk(-sn[0], cs[0]), pk(-sn[1], cs[1]), pk(-sn[2], cs[2]), pk(-sn[3], cs[3])};
  const u32 c0 = pk(cs[0], cs[1]), c1 = pk(cs[2], cs[3]), s0 = pk(sn[0], sn[1]), s1 = pk(sn[2], sn[3]);
  const u32x4 Fr = {c0, c1, s0, s1}, Fi = {s0 ^ SGN_BOTH, s1 ^ SGN_BOTH, c0, c1};
  const u32x4 Ir = {c0, c1, s0 ^ SGN_BOTH, s1 ^ SGN_BOTH}, Ii = {s0, s1, c0, c1};
  u32 o[2][4];
#pragma unroll
  for (int e = 0; e < 2; ++e) {
    const u32x4 a = {u[0][e], u[1][e], u[2][e], u[3][e]};
    f32x4 dr = mma32(a, B1r, Z4), di = mma32(a, B1i, Z4), re, im;
#pragma unroll
    for (int i = 0; i < 4; ++i) { re[i] = dr[i] * tc[i] + di[i] * ts[i]; im[i] = di[i] * tc[i] - dr[i] * ts[i]; }
    u32x4 b = packc(re, im);
    dr = mma32(Fr, b, Z4); di = mma32(Fi, b, Z4);
#pragma unroll
    for (int i = 0; i < 4; ++i) { const float wr = blo(wt[e][i]), wi = bhi(wt[e][i]); re[i] = dr[i] * wr - di[i] * wi; im[i] = di[i] * wr + dr[i] * wi; }
    b = packc(re, im);
    dr = mma32(b, Ir, Z4); di = mma32(b, Ii, Z4);
#pragma unroll
    for (int i = 0; i < 4; ++i) { re[i] = dr[i] * tc[i] - di[i] * ts[i]; im[i] = di[i] * tc[i] + dr[i] * ts[i]; }
    b = packc(re, im);
    dr = mma32(Ir, b, Z4); di = mma32(Ii, b, Z4);
#pragma unroll
    for (int i = 0; i < 4; ++i) o[e][i] = pk(dr[i], di[i]);
  }
#pragma unroll
  for (int i = 0; i < 4; ++i) L[(16 * (4 * g + i) + c) * 4 + w] = u32x2{o[0][i], o[1][i]};
  __syncthreads();
#pragma unroll
  for (int r = 0; r < 2; ++r) {
    const int p = threadIdx.x + 256 * r;
    Y[((p >> 1) * 256 + k) * 2 + (p & 1)] = reinterpret_cast<const u32x4*>(L)[p];
  }
}

__global__ __launch_bounds__(256) void k_a3(const u32x2* __restrict__ Y, const u16* __restrict__ x, u16* __restrict__ x1, u32x4* __restrict__ XN,
                                            const u16* __restrict__ wd, u32x4* __restrict__ WD) {
  __shared__ u32x2 L[4][264];
  if (blockIdx.x >= 256) { pre_dn(blockIdx.x - 256, wd, WD); return; }
  const int s1 = (blockIdx.x & 7) * 32 + (blockIdx.x >> 3);
  const int w = threadIdx.x >> 6, l = threadIdx.x & 63, c = l & 15, g = l >> 4;
  u32x2 y[4], xr[2][2];
#pragma unroll
  for (int u = 0; u < 4; ++u) y[u] = Y[(s1 * 256 + c + 16 * (4 * w + u)) * 4 + g];
#pragma unroll
  for (int e = 0; e < 2; ++e)
#pragma unroll
    for (int t = 0; t < 2; ++t) xr[e][t] = *reinterpret_cast<const u32x2*>(x + (s1 + 256 * (2 * w + e)) * 512 + 256 * t + 16 * c + 4 * g);
  u32x2 G, Br, Bi; u32x4 B2[2][2]; float tc[2][4], ts[2][4];
#pragma unroll
  for (int q = 0; q < 2; ++q) {
    const float a = (float)(((2 * g + q) * (s1 + 256 * (c >> 1))) & 2047) * (1.f / 2048), cc = cosr(a), ss = sinr(a);
    G[q] = (c & 1) ? pk(ss, cc) : pk(cc, -ss);
    const float b = (float)(((2 * g + q) * c) & 15) * (1.f / 16), cb = cosr(b), sb = sinr(b);
    Br[q] = pk(cb, -sb); Bi[q] = pk(sb, cb);
  }
#pragma unroll
  for (int t = 0; t < 2; ++t)
#pragma unroll
    for (int i = 0; i < 4; ++i) { const float a = (float)((16 * t + 4 * g + i) * c) * (1.f / 512); tc[t][i] = cosr(a); ts[t][i] = sinr(a); }
  {
    float cs[4], sn[4];
#pragma unroll
    for (int j = 0; j < 4; ++j) { const float a = (float)(((4 * g + j) * c) & 31) * (1.f / 32); cs[j] = cosr(a); sn[j] = -sinr(a); }
    B2[0][0] = u32x4{pk(cs[0], cs[1]), pk(cs[2], cs[3]), pk(sn[0], sn[1]), pk(sn[2], sn[3])};
    const u32 f = (c & 1) ? SGN_BOTH : 0u;
    B2[0][1] = B2[0][0] ^ SGN_HI; B2[1][0] = B2[0][0] ^ f; B2[1][1] = B2[0][1] ^ f;
  }
#pragma unroll
  for (int u = 0; u < 4; ++u) {
    const f32x4 o = mma16(G, y[u], Z4);
    L[g][c + 16 * (4 * w + u)] = u32x2{pk(o[0], o[1]), pk(o[2], o[3])};
  }
  __syncthreads();
  u32x2 r[2][2];
#pragma unroll
  for (int t = 0; t < 2; ++t)
#pragma unroll
    for (int q = 0; q < 2; ++q) r[t][q] = L[w][c + 16 * t + 32 * (2 * g + q)];
#pragma unroll
  for (int e = 0; e < 2; ++e) {
    const float nyq = blo(__builtin_amdgcn_readfirstlane(r[0][0][e]));
    const u32 r00 = l == 0 ? 0u : r[0][0][e];
    f32x4 acc[2];
#pragma unroll
    for (int t = 0; t < 2; ++t) acc[t] = f32x4{blo(xr[e][t][0]), bhi(xr[e][t][0]), blo(xr[e][t][1]), bhi(xr[e][t][1])};
#pragma unroll
    for (int t = 0; t < 2; ++t) {
      const u32x2 a = {t == 0 ? r00 : r[1][0][e], r[t][1][e]};
      f32x4 dr = mma16(a, Br, Z4), di = mma16(a, Bi, Z4), re, im;
      if (t == 0) dr[0] += g == 0 ? ((c & 1) ? -nyq : nyq) : 0.f;
#pragma unroll
      for (int i = 0; i < 4; ++i) { re[i] = dr[i] * tc[t][i] - di[i] * ts[t][i]; im[i] = di[i] * tc[t][i] + dr[i] * ts[t][i]; }
      const u32x4 a2 = packc(re, im);
      acc[0] = mma32(a2, B2[t][0], acc[0]); acc[1] = mma32(a2, B2[t][1], acc[1]);
    }
    const u32x4 xo = packc(acc[0], acc[1]);
    u16* xp = x1 + (s1 + 256 * (2 * w + e)) * 512 + 16 * c + 4 * g;
    *reinterpret_cast<u32x2*>(xp) = u32x2{xo[0], xo[1]};
    *reinterpret_cast<u32x2*>(xp + 256) = u32x2{xo[2], xo[3]};
    float v[8], s = 0.f;
#pragma unroll
    for (int p = 0; p < 4; ++p) { v[2 * p] = blo(xo[p]); v[2 * p + 1] = bhi(xo[p]); s += v[2 * p] + v[2 * p + 1]; }
    const float mean = wave_sum(s) * (1.f / 512);
    float q = 0.f;
#pragma unroll
    for (int j = 0; j < 8; ++j) { v[j] -= mean; q += v[j] * v[j]; }
    const float rs = __builtin_amdgcn_rsqf(wave_sum(q) * (1.f / 512) + 1e-5f);
    const int tau = 8 * s1 + 2 * w + e;
    XN[(tau >> 5) * 2048 + l * 32 + (tau & 31)] = u32x4{pk(v[0] * rs, v[1] * rs), pk(v[2] * rs, v[3] * rs), pk(v[4] * rs, v[5] * rs), pk(v[6] * rs, v[7] * rs)};
  }
}

DEV void glds16(const void* gsrc, u32 lds_dst) {
  u32 keep;
  asm volatile("s_mov_b32 %0, m0\n\ts_mov_b32 m0, %2\n\ts_nop 0\n\tglobal_load_lds_dwordx4 %1, off\n\ts_mov_b32 m0, %0" : "=&s"(keep) : "v"(gsrc), "s"(lds_dst) : "memory");
}
#define GLD(dst, ptr, off) asm volatile("global_load_dwordx4 %0, %1, off offset:%2" : "=v"(dst) : "v"(ptr), "i"(off))
template <int N> DEV void waitx(u32x4& a, u32x4& b) { asm volatile("s_waitcnt vmcnt(%2)" : "+v"(a), "+v"(b) : "i"(N) : "memory"); }

__global__ __launch_bounds__(512, 2) void k_b1(const u32x4* __restrict__ XN, const u32x4* __restrict__ WG, u32x4* __restrict__ H) {
  extern __shared__ __attribute__((aligned(16))) u32x4 SM[];
  const int w = __builtin_amdgcn_readfirstlane(threadIdx.x >> 6), l = threadIdx.x & 63;
  const int T = (blockIdx.x & 7) * 8 + w, F0 = (blockIdx.x >> 3) * 3;
  const u32 lbase = (u32)(size_t)(__attribute__((address_space(3))) u32x4*)SM;
  const u32x4* xp = XN + T * 2048 + l;
  const u32x4* wp[3];
#pragma unroll
  for (int i = 0; i < 3; ++i) wp[i] = WG + (min(F0 + i, NF - 1) * 32 + w) * 64 + l;
  u32x4 xf[32];
#pragma unroll
  for (int j = 0; j < 4; ++j) {
#pragma unroll
    for (int i = 0; i < 3; ++i) glds16(wp[i] + j * 512, lbase + ((8 * j + w) * 3 + i) * 1024);
    const u32x4* xq = xp + j * 512 + 256;
    GLD(xf[8 * j + 0], xq, -4096); GLD(xf[8 * j + 1], xq, -3072); GLD(xf[8 * j + 2], xq, -2048); GLD(xf[8 * j + 3], xq, -1024);
    GLD(xf[8 * j + 4], xq, 0); GLD(xf[8 * j + 5], xq, 1024); GLD(xf[8 * j + 6], xq, 2048); GLD(xf[8 * j + 7], xq, 3072);
  }
  f32x16 acc[3] = {};
#define B1STEP(ks) { _Pragma("unroll") for (int i = 0; i < 3; ++i) acc[i] = mmb(SM[((ks) * 3 + i) * 64 + l], xf[ks], acc[i]); }
#define B1STAGE(j) { waitx<39 - 11 * j>(xf[8 * j], xf[8 * j + 1]); __builtin_amdgcn_s_barrier(); asm volatile("" ::: "memory"); \
    B1STEP(8 * j) B1STEP(8 * j + 1) waitx<37 - 11 * j>(xf[8 * j + 2], xf[8 * j + 3]); B1STEP(8 * j + 2) B1STEP(8 * j + 3) \
    waitx<35 - 11 * j>(xf[8 * j + 4], xf[8 * j + 5]); B1STEP(8 * j + 4) B1STEP(8 * j + 5) \
    waitx<33 - 11 * j>(xf[8 * j + 6], xf[8 * j + 7]); B1STEP(8 * j + 6) B1STEP(8 * j + 7) }
  B1STAGE(0) B1STAGE(1) B1STAGE(2) B1STAGE(3)
#pragma unroll
  for (int i = 0; i < 3; ++i) {
    float h[8];
#pragma unroll
    for (int r = 0; r < 8; ++r) h[r] = fmaxf(acc[i][r], 0.f) * acc[i][r + 8];
    if (F0 + i < NF) H[(T * NF + F0 + i) * 64 + l] = u32x4{pk(h[0], h[1]), pk(h[2], h[3]), pk(h[4], h[5]), pk(h[6], h[7])};
  }
}

__global__ __launch_bounds__(256, 1) void k_b2(const u32x4* __restrict__ H, const u32x4* __restrict__ WD, const u16* __restrict__ x1, u16* __restrict__ xo) {
  __shared__ f32x4 P[4][3][4][64];
  const int w = __builtin_amdgcn_readfirstlane(threadIdx.x >> 6), l = threadIdx.x & 63;
  const int r_ = blockIdx.x >> 3, TG = (blockIdx.x & 7) * 4 + (r_ >> 3), NG = r_ & 7;
  const int f0 = w * 22 - (w == 3);
  const u32x4* hp = H + (2 * TG * NF + f0) * 64 + l;
  const u32x4* dp = WD + (2 * NG * NF + f0) * 64 + l;
  u32x4 a[22][2], b[22][2];
#pragma unroll
  for (int s = 0; s < 22; ++s) {
    const int o = (s == 21 && w >= 2) ? 20 * 64 : s * 64;
    a[s][0] = dp[o]; b[s][0] = hp[o]; a[s][1] = dp[NF * 64 + o]; b[s][1] = hp[NF * 64 + o];
  }
  const int tau = (2 * TG + (w >> 1)) * 32 + (l & 31), srow = (tau >> 3) + 256 * (tau & 7);
  const int col = (2 * NG + (w & 1)) * 32 + 16 * (l >> 5);
  const u32x4 r0 = *reinterpret_cast<const u32x4*>(x1 + srow * 512 + col), r1 = *reinterpret_cast<const u32x4*>(x1 + srow * 512 + col + 8);
  __builtin_amdgcn_sched_barrier(0);
  f32x16 acc[4] = {};
#pragma unroll
  for (int s = 0; s < 22; ++s) {
    if (s == 21 && w >= 2) break;
#pragma unroll
    for (int q = 0; q < 4; ++q) acc[q] = mmb(a[s][q & 1], b[s][q >> 1], acc[q]);
  }
  f32x16 own;
#pragma unroll
  for (int q = 0; q < 4; ++q) {
    if (q == w) own = acc[q];
    else {
      const int slot = (w - q - 1) & 3;
#pragma unroll
      for (int p = 0; p < 4; ++p) P[q][slot][p][l] = f32x4{acc[q][4 * p], acc[q][4 * p + 1], acc[q][4 * p + 2], acc[q][4 * p + 3]};
    }
  }
  __syncthreads();
#pragma unroll
  for (int sl = 0; sl < 3; ++sl)
#pragma unroll
    for (int p = 0; p < 4; ++p) { const f32x4 v = P[w][sl][p][l]; own[4 * p] += v[0]; own[4 * p + 1] += v[1]; own[4 * p + 2] += v[2]; own[4 * p + 3] += v[3]; }
  u32x4 o0, o1;
#pragma unroll
  for (int p = 0; p < 4; ++p) {
    o0[p] = pk(own[2 * p] + blo(r0[p]), own[2 * p + 1] + bhi(r0[p]));
    o1[p] = pk(own[8 + 2 * p] + blo(r1[p]), own[9 + 2 * p] + bhi(r1[p]));
  }
  *reinterpret_cast<u32x4*>(xo + srow * 512 + col) = o0;
  *reinterpret_cast<u32x4*>(xo + srow * 512 + col + 8) = o1;
}

extern "C" void kernel_launch(void* const* d_in, const int* in_sizes, int n_in, void* d_out, int out_size,
                              void* d_ws, size_t ws_size, hipStream_t stream) {
  const u16 *x = (const u16*)d_in[0], *wr = (const u16*)d_in[1], *wi = (const u16*)d_in[2];
  const u16 *wg = (const u16*)d_in[4], *wu = (const u16*)d_in[5], *wd = (const u16*)d_in[6];
  char* ws = (char*)d_ws;
  const size_t MB = 1 << 20;
  u32x2* Z = (u32x2*)ws; u32x4* Y = (u32x4*)(ws + 2 * MB); u32x4* WT = (u32x4*)(ws + 4 * MB);
  u16* X1 = (u16*)(ws + 6 * MB); u16* X2 = (u16*)(ws + 8 * MB); u32x4* XN = (u32x4*)(ws + 10 * MB);
  u32x4* WG = (u32x4*)(ws + 12 * MB); u32x4* WD = (u32x4*)(ws + 15 * MB); u32x4* H = (u32x4*)(ws + 17 * MB);
  (void)hipFuncSetAttribute((const void*)k_b1, hipFuncAttributeMaxDynamicSharedMemorySize, 98304);
  for (int L = 0; L < 2; ++L) {
    const u16* xin = L ? X2 : x;
    k_a1<<<L ? 256 : 256 + 512, 256, 0, stream>>>(xin, Z, wr, wi, WT);
    k_a2<<<L ? 256 : 256 + 22 * 32, 256, 0, stream>>>(Z, WT, Y, wg, wu, WG);
    k_a3<<<L ? 256 : 256 + 4 * NF, 256, 0, stream>>>((const u32x2*)Y, xin, X1, XN, wd, WD);
    k_b1<<<232, 512, 98304, stream>>>(XN, WG, H);
    k_b2<<<256, 256, 0, stream>>>(H, WD, X1, L ? (u16*)d_out : X2);
  }
}
```

```cpp
#include <hip/hip_runtime.h>
#include <hip/hip_bf16.h>

typedef unsigned short u16;
typedef unsigned int u32;
typedef float f32x2 __attribute__((ext_vector_type(2)));
typedef float f32x4 __attribute__((ext_vector_type(4)));
typedef float f32x16 __attribute__((ext_vector_type(16)));
typedef u32 u32x2 __attribute__((ext_vector_type(2)));
typedef u32 u32x4 __attribute__((ext_vector_type(4)));
typedef __bf16 bf2 __attribute__((ext_vector_type(2)));
typedef __bf16 bf8 __attribute__((ext_vector_type(8)));
typedef short s16x4 __attribute__((ext_vector_type(4)));

#define DEV __device__ __forceinline__
#define DARG
#define DPASS(n)
#define STAMP(i)
#define FLUSH(m)
#define VW()
constexpr int NF = 86;
constexpr u32 SGN_HI = 0x80000000u, SGN_LO = 0x00008000u, SGN_BOTH = 0x80008000u;

DEV u32 pk(float a, float b) { f32x2 v = {a, b}; return __builtin_bit_cast(u32, __builtin_convertvector(v, bf2)); }
DEV float blo(u32 v) { return __builtin_bit_cast(float, v << 16); }
DEV float bhi(u32 v) { return __builtin_bit_cast(float, v & 0xffff0000u); }
DEV float cosr(float r) { return __builtin_amdgcn_cosf(r); }
DEV float sinr(float r) { return __builtin_amdgcn_sinf(r); }
DEV f32x4 mma32(u32x4 a, u32x4 b, f32x4 c) { return __builtin_amdgcn_mfma_f32_16x16x32_bf16(__builtin_bit_cast(bf8, a), __builtin_bit_cast(bf8, b), c, 0, 0, 0); }
DEV f32x4 mma16(u32x2 a, u32x2 b, f32x4 c) { return __builtin_amdgcn_mfma_f32_16x16x16bf16_1k(__builtin_bit_cast(s16x4, a), __builtin_bit_cast(s16x4, b), c, 0, 0, 0); }
DEV f32x16 mmb(u32x4 a, u32x4 b, f32x16 c) { return __builtin_amdgcn_mfma_f32_32x32x16_bf16(__builtin_bit_cast(bf8, a), __builtin_bit_cast(bf8, b), c, 0, 0, 0); }
template <int CTRL> DEV float dpp(float v) { return __builtin_bit_cast(float, __builtin_amdgcn_update_dpp(0, __builtin_bit_cast(int, v), CTRL, 0xf, 0xf, true)); }
DEV float wave_sum(float v) {
  v += dpp<0xB1>(v); v += dpp<0x4E>(v); v += dpp<0x141>(v); v += dpp<0x140>(v);
  u32 u = __builtin_bit_cast(u32, v);
  auto r = __builtin_amdgcn_permlane16_swap(u, u, false, false);
  const u32 r0 = r[0], r1 = r[1];
  v = __builtin_bit_cast(float, r0) + __builtin_bit_cast(float, r1);
  u = __builtin_bit_cast(u32, v);
  auto q = __builtin_amdgcn_permlane32_swap(u, u, false, false);
  const u32 q0 = q[0], q1 = q[1];
  return __builtin_bit_cast(float, q0) + __builtin_bit_cast(float, q1);
}
DEV u32x4 packc(const f32x4& re, const f32x4& im) { return u32x4{pk(re[0], re[1]), pk(re[2], re[3]), pk(im[0], im[1]), pk(im[2], im[3])}; }
const f32x4 __device__ Z4 = {0.f, 0.f, 0.f, 0.f};

DEV void pre_filter(int pb, int tid, const u16* wr, const u16* wi, u32x4* WT) {
  const int kb = pb & 7, r = pb >> 3, m2 = r >> 3, g = (r >> 1) & 3, c = 8 * (r & 1) + (tid & 7);
  const int k = 32 * kb + (tid >> 3), kk = k ? k : 256;
  const float sc = k ? 0x1p-19f : 0x1p-20f;
  u32x4 o;
#pragma unroll
  for (int i = 0; i < 4; ++i) {
    const int m = m2 + 8 * (c + 16 * (4 * g + i));
    o[i] = pk(blo(wr[m * 257 + kk]) * sc, blo(wi[m * 257 + kk]) * sc);
  }
  WT[(k * 8 + m2) * 64 + 16 * g + c] = o;
}
DEV void pre_gu(int pb, int tid, const u16* wg, const u16* wu, u32x4* WG) {
  const int l = tid & 63, F = (pb >> 5) * 4 + (tid >> 6), ks = pb & 31;
  if (F >= NF) return;
  const int r = l & 31, lam = 2 * ks + (l >> 5), d0 = 16 * (lam & 15) + 4 * (lam >> 4);
  const u16* src = (r < 16 ? wg : wu) + 16 * F + (r & 15);
  u32 v[8];
#pragma unroll
  for (int j = 0; j < 8; ++j) v[j] = src[(d0 + (j & 3) + 256 * (j >> 2)) * 1376];
  WG[(F * 32 + ks) * 64 + l] = u32x4{v[0] | v[1] << 16, v[2] | v[3] << 16, v[4] | v[5] << 16, v[6] | v[7] << 16};
}
DEV void pre_dn(int pb, int tid, const u16* wd, u32x4* WD) {
  const int l = tid & 63, nt = (pb & 3) * 4 + (tid >> 6), F = pb >> 2;
  const int r = l & 31, h = l >> 5, n = 32 * nt + 16 * ((r >> 2) & 1) + (r & 3) + 4 * (r >> 3);
  u32 v[8];
#pragma unroll
  for (int j = 0; j < 8; ++j) v[j] = F < NF ? wd[(16 * F + (j & 3) + 8 * (j >> 2) + 4 * h) * 512 + n] : 0;
  WD[(nt * 88 + F) * 64 + l] = u32x4{v[0] | v[1] << 16, v[2] | v[3] << 16, v[4] | v[5] << 16, v[6] | v[7] << 16};
}

constexpr u32 MB = 1u << 20;
constexpr u32 O_Z1 = 0, O_Y1 = 2 * MB, O_Z2 = 4 * MB, O_Y2 = 6 * MB, O_X2 = 8 * MB, O_WT = 10 * MB, O_X1 = 12 * MB  ,
              O_XN = 16 * MB  , O_WG = 20 * MB, O_WD = 23 * MB, O_CNT = 25 * MB, O_H = 26 * MB  , WS_END = 38 * MB;
constexpr u32 SENT = 0xFFFFFFFFu;
constexpr int SPIN_CAP = 1 << 18;
typedef __amdgpu_buffer_rsrc_t rsrc_t;
typedef __attribute__((address_space(1))) u32 gu32;
DEV rsrc_t mkrs(const void* p, u32 n) { return __builtin_amdgcn_make_buffer_rsrc(const_cast<void*>(p), 0, n, 0x00020000); }
template <int AUX> DEV u32x4 ld4(rsrc_t r, u32 v, u32 s) { return __builtin_amdgcn_raw_buffer_load_b128(r, v, s, AUX); }
template <int AUX> DEV u32x2 ld2(rsrc_t r, u32 v, u32 s) { return __builtin_amdgcn_raw_buffer_load_b64(r, v, s, AUX); }
template <int AUX> DEV void st4(u32x4 d, rsrc_t r, u32 v, u32 s) { __builtin_amdgcn_raw_buffer_store_b128(d, r, v, s, AUX); }
template <int AUX> DEV void st2(u32x2 d, rsrc_t r, u32 v, u32 s) { __builtin_amdgcn_raw_buffer_store_b64(d, r, v, s, AUX); }
DEV u32 san(u32 v) { return min(v, SENT - 1u); }
DEV u32 mx4(u32x4 v) { return max(max(v[0], v[1]), max(v[2], v[3])); }
#define PIN(x) asm volatile("" : "+v"(x))
#define LANE() int l = tid0 & 63; PIN(l); const int c = l & 15, g = l >> 4, tid = w * 64 + l; (void)c; (void)g; (void)tid
#define SB() __builtin_amdgcn_sched_barrier(0)
#define DRAIN() asm volatile("s_waitcnt vmcnt(0)" ::: "memory")
DEV u32x4 poll4(rsrc_t r, u32 v, u32 s, int& cap) {
  u32x4 d; int n = 0;
  do { asm volatile("" ::: "memory"); d = ld4<16>(r, v, s); } while (__any(mx4(d) == SENT) && ++n < cap);
  if (n >= cap) cap = 0;
  return d;
}
DEV void arrive(gu32* c) { DRAIN(); __syncthreads(); if (threadIdx.x == 0) __hip_atomic_fetch_add(c, 1u, __ATOMIC_RELAXED, __HIP_MEMORY_SCOPE_AGENT); }
DEV void await(gu32* c, u32 n, int& cap, int w) {
  if (w == 0) { int k = 0; while ((u32)__builtin_amdgcn_readfirstlane(__hip_atomic_load(c, __ATOMIC_RELAXED, __HIP_MEMORY_SCOPE_AGENT)) < n && ++k < cap) __builtin_amdgcn_s_sleep(1); if (k >= cap) cap = 0; }
  __syncthreads();
}

template <bool POLL> DEV void phase_a1(rsrc_t rx, u32 xs, rsrc_t rw, u32 zs, char* scr, int s1, int w, int l, int& cap) {
  constexpr int STO = 64512; (void)STO;
  const int c = l & 15, g = l >> 4;
  const u32 xo = (s1 + 256 * w) * 1024 + l * 16;
  u32x4 xd;
  if (!POLL) xd = ld4<0>(rx, xo, xs);
  u32x4 Bc, Bs, Am; u32x2 G; float tc[2][4], ts[2][4];
#pragma unroll
  for (int p = 0; p < 4; ++p) {
    const float a0 = (float)(((8 * g + 2 * p) * c) & 31) * (1.f / 32), a1 = (float)(((8 * g + 2 * p + 1) * c) & 31) * (1.f / 32);
    Bc[p] = pk(cosr(a0), cosr(a1)); Bs[p] = pk(-sinr(a0), -sinr(a1));
  }
#pragma unroll
  for (int t = 0; t < 2; ++t)
#pragma unroll
    for (int i = 0; i < 4; ++i) { const float a = (float)((c + 16 * t) * (4 * g + i)) * (1.f / 512); tc[t][i] = cosr(a); ts[t][i] = sinr(a); PIN(tc[t][i]); PIN(ts[t][i]); }
  {
    float cs[4], sn[4];
#pragma unroll
    for (int j = 0; j < 4; ++j) { const float a = (float)(((4 * g + j) * (c >> 1)) & 15) * (1.f / 16); cs[j] = cosr(a); sn[j] = sinr(a); }
    const u32 c0 = pk(cs[0], cs[1]), c1 = pk(cs[2], cs[3]), s0 = pk(sn[0], sn[1]), s1_ = pk(sn[2], sn[3]);
    Am = (c & 1) ? u32x4{s0 ^ SGN_BOTH, s1_ ^ SGN_BOTH, c0, c1} : u32x4{c0, c1, s0, s1_};
#pragma unroll
    for (int q = 0; q < 2; ++q) {
      const float a = (float)(((c >> 1) * (s1 + 256 * (2 * g + q))) & 2047) * (1.f / 2048), cc = cosr(a), ss = sinr(a);
      G[q] = (c & 1) ? pk(-ss, cc) : pk(cc, ss);
    }
  }
  PIN(Bc); PIN(Bs); PIN(Am); PIN(G); SB();
  if (POLL) { xd = poll4(rx, xo, xs, cap); STAMP(10); }
  char* tr = scr + 16384 + w * 1280;
  *reinterpret_cast<u32x4*>(tr + g * 288 + c * 16) = xd;
  float v[8], s = 0.f;
#pragma unroll
  for (int p = 0; p < 4; ++p) s += blo(xd[p]) + bhi(xd[p]);
  __builtin_amdgcn_fence(__ATOMIC_RELEASE, "wavefront"); __builtin_amdgcn_wave_barrier(); __builtin_amdgcn_fence(__ATOMIC_ACQUIRE, "wavefront");
#pragma unroll
  for (int j = 0; j < 8; ++j) v[j] = blo(*reinterpret_cast<const u16*>(tr + g * 288 + j * 32 + c * 2));
  const float mean = wave_sum(s) * (1.f / 512);
  float q = 0.f;
#pragma unroll
  for (int j = 0; j < 8; ++j) { v[j] -= mean; q += v[j] * v[j]; }
  const float rs = __builtin_amdgcn_rsqf(wave_sum(q) * (1.f / 512) + 1e-5f);
  const u32x4 a = {pk(v[0] * rs, v[1] * rs), pk(v[2] * rs, v[3] * rs), pk(v[4] * rs, v[5] * rs), pk(v[6] * rs, v[7] * rs)};
  const u32 ny = c == 0 ? 0xBF803F80u : 0u;
  u32* L = reinterpret_cast<u32*>(scr);
#pragma unroll
  for (int t = 0; t < 2; ++t) {
    const u32 f = t ? SGN_HI : 0u;
    const f32x4 dr = mma32(a, Bc ^ f, Z4), di = mma32(a, Bs ^ f, Z4);
    f32x4 re, im;
#pragma unroll
    for (int i = 0; i < 4; ++i) { re[i] = dr[i] * tc[t][i] + di[i] * ts[t][i]; im[i] = di[i] * tc[t][i] - dr[i] * ts[t][i]; }
    const u32x4 b = packc(re, im);
    f32x4 R = mma32(Am, b, Z4);
    if (t == 0) { const f32x4 n = mma32(u32x4{ny, ny, 0u, 0u}, b, Z4); if (l == 0) { R[0] = n[0]; R[1] = 0.f; } }
#pragma unroll
    for (int q2 = 0; q2 < 2; ++q2) L[((w >> 1) * 264 + c + 16 * t + 32 * (2 * g + q2)) * 2 + (w & 1)] = pk(R[2 * q2], R[2 * q2 + 1]);
  }
  __syncthreads();
#pragma unroll
  for (int u = 0; u < 2; ++u) {
    const int k = c + 16 * (2 * w + u);
    const f32x4 o = mma16(G, reinterpret_cast<const u32x2*>(L)[g * 264 + k], Z4);
    st2<POLL ? 16 : 0>(u32x2{san(pk(o[0], o[1])), san(pk(o[2], o[3]))}, rw, (k * 256 + s1) * 32 + g * 8, zs);
  }
}

__global__ __launch_bounds__(512, 2) void k_pre(const u16* __restrict__ x, const u16* __restrict__ wr, const u16* __restrict__ wi, const u16* __restrict__ wg,
                                                const u16* __restrict__ wu, const u16* __restrict__ wd, char* __restrict__ ws DARG) {
  __shared__ __attribute__((aligned(16))) char scr[16384 + 8 * 1280 + 256];
  constexpr int STO = 26624; (void)STO;
  const int b = blockIdx.x, tid = threadIdx.x;
  if (b >= 256) {
    const int p = (b - 256) * 2 + (tid >> 8), t = tid & 255;
    if (p < 512) pre_filter(p, t, wr, wi, (u32x4*)(ws + O_WT));
    else if (p < 512 + 704) pre_gu(p - 512, t, wg, wu, (u32x4*)(ws + O_WG));
    else pre_dn(p - 1216, t, wd, (u32x4*)(ws + O_WD));
    return;
  }
  STAMP(0);
  const rsrc_t rw = mkrs(ws, WS_END), rx = mkrs(x, 2 * MB);
#pragma unroll
  for (int i = 0; i < 4; ++i) st4<16>(u32x4{SENT, SENT, SENT, SENT}, rw, (b * 512 + tid) * 16, O_Y1 + i * 2 * MB);
  if (b == 0 && tid < 32) *reinterpret_cast<u32*>(ws + O_CNT + tid * 256) = 0u;
  int cap = 0;
  phase_a1<false>(rx, 0, rw, O_Z1, scr, (b & 7) * 32 + (b >> 3), __builtin_amdgcn_readfirstlane(tid >> 6), tid & 63, cap);
  STAMP(1); VW(); STAMP(4); FLUSH(19);
}

DEV void glds16(const void* gsrc, u32 lds_dst) {
  u32 keep;
  asm volatile("s_mov_b32 %0, m0\n\ts_mov_b32 m0, %2\n\ts_nop 0\n\tglobal_load_lds_dwordx4 %1, off\n\ts_mov_b32 m0, %0" : "=&s"(keep) : "v"(gsrc), "s"(lds_dst) : "memory");
}
constexpr int XW = 10, HW = 5;

__global__ __launch_bounds__(512, 2) void k_main(const u16* __restrict__ x, char* __restrict__ ws, u16* __restrict__ out DARG) {
  extern __shared__ __attribute__((aligned(16))) u32x4 SM[];
  char* scr = reinterpret_cast<char*>(SM) + 98304;
  constexpr int STO = 64512; (void)STO;
  const int b = blockIdx.x, tid0 = threadIdx.x, w = __builtin_amdgcn_readfirstlane(tid0 >> 6);
  const int grp = b & 7, r_ = b >> 3, s1 = grp * 32 + r_;
  const rsrc_t rw = mkrs(ws, WS_END), rx0 = mkrs(x, 2 * MB), ro = mkrs(out, 2 * MB);
  int cap = SPIN_CAP;
  STAMP(0);
  const u32x4 wt = ld4<0>(rw, tid0 * 16, O_WT + s1 * 8192);
  const int NG = r_ & 7, TG = grp * 4 + (r_ >> 3), F0 = r_ * 3, rot = (r_ * 9) & 31, rot2 = (NG * 11) >> 3;
  u32x4 wd[11][2];
#define FSTEP(s) (11 * w + (s) + rot2 - ((s) + rot2 >= 11 ? 11 : 0))
#define LDWD(s) { const u32 o_ = O_WD + (2 * NG * 88 + FSTEP(s)) * 1024; wd[s][0] = ld4<0>(rw, l * 16, o_); wd[s][1] = ld4<0>(rw, l * 16, o_ + 88 * 1024); }
  const u32 lb = (u32)(size_t)(__attribute__((address_space(3))) u32x4*)SM;
#define LDGU(i) if (r_ < 29) { const char* src = ws + O_WG + (min(F0 + (i), NF - 1) * 32 + w * 4) * 1024 + l * 16; \
    _Pragma("unroll") for (int j = 0; j < 4; ++j) glds16(src + j * 1024, lb + (((4 * w + j - rot) & 31) * 3 + (i)) * 1024); }

  for (int L = 0; L < 2; ++L) {
    {
      LANE();
      u32x4 zd;
      if (L == 0) zd = ld4<0>(rw, tid * 16, O_Z1 + s1 * 8192);
      float cs[4], sn[4], tc[4], ts[4];
#pragma unroll
      for (int j = 0; j < 4; ++j) {
        const int p = (4 * g + j) * c;
        const float a = (float)(p & 15) * (1.f / 16), bb = (float)p * (1.f / 256);
        cs[j] = cosr(a); sn[j] = sinr(a); tc[j] = cosr(bb); ts[j] = sinr(bb); PIN(tc[j]); PIN(ts[j]);
      }
      u32x4 B1r = {pk(cs[0], sn[0]), pk(cs[1], sn[1]), pk(cs[2], sn[2]), pk(cs[3], sn[3])};
      u32x4 B1i = {pk(-sn[0], cs[0]), pk(-sn[1], cs[1]), pk(-sn[2], cs[2]), pk(-sn[3], cs[3])};
      u32x4 Fr = {pk(cs[0], cs[1]), pk(cs[2], cs[3]), pk(sn[0], sn[1]), pk(sn[2], sn[3])};
      PIN(B1r); PIN(B1i); PIN(Fr); SB();
      if (L == 1) zd = poll4(rw, tid * 16, O_Z2 + s1 * 8192, cap);
      u32* P = reinterpret_cast<u32*>(scr + 8448);
#pragma unroll
      for (int i = 0; i < 4; ++i) { const int sa = tid >> 1; P[(4 * (tid & 1) + i) * 312 + sa + 16 * (sa >> 6)] = zd[i]; }
      if (L == 0) LDGU(0)
      asm volatile("s_waitcnt lgkmcnt(0)" ::: "memory"); __builtin_amdgcn_s_barrier(); asm volatile("" ::: "memory");
      STAMP(1 + 12 * L);
      u32x4 a;
#pragma unroll
      for (int j = 0; j < 4; ++j) a[j] = P[w * 312 + 80 * g + 16 * j + c];
      const u32x4 Fi = {Fr[2] ^ SGN_BOTH, Fr[3] ^ SGN_BOTH, Fr[0], Fr[1]}, Ir = {Fr[0], Fr[1], Fi[0], Fi[1]}, Ii = {Fr[2], Fr[3], Fr[0], Fr[1]};
      f32x4 dr = mma32(a, B1r, Z4), di = mma32(a, B1i, Z4), re, im;
#pragma unroll
      for (int i = 0; i < 4; ++i) { re[i] = dr[i] * tc[i] + di[i] * ts[i]; im[i] = di[i] * tc[i] - dr[i] * ts[i]; }
      u32x4 bb = packc(re, im);
      dr = mma32(Fr, bb, Z4); di = mma32(Fi, bb, Z4);
#pragma unroll
      for (int i = 0; i < 4; ++i) { const float wr = blo(wt[i]), wi = bhi(wt[i]); re[i] = dr[i] * wr - di[i] * wi; im[i] = di[i] * wr + dr[i] * wi; }
      bb = packc(re, im);
      dr = mma32(bb, Ir, Z4); di = mma32(bb, Ii, Z4);
#pragma unroll
      for (int i = 0; i < 4; ++i) { re[i] = dr[i] * tc[i] - di[i] * ts[i]; im[i] = di[i] * tc[i] + dr[i] * ts[i]; }
      bb = packc(re, im);
      dr = mma32(Ir, bb, Z4); di = mma32(Ii, bb, Z4);
      u32* Q = reinterpret_cast<u32*>(scr + 20480);
#pragma unroll
      for (int i = 0; i < 4; ++i) Q[(16 * (4 * g + i) + c) * 8 + (w ^ (((c >> 2) & 1) * 4))] = san(pk(dr[i], di[i]));
      asm volatile("s_waitcnt lgkmcnt(0)" ::: "memory"); __builtin_amdgcn_s_barrier(); asm volatile("" ::: "memory");
      st4<16>(reinterpret_cast<const u32x4*>(Q)[tid ^ ((tid >> 3) & 1)], rw, ((tid >> 1) * 256 + s1) * 32 + (tid & 1) * 16, L ? O_Y2 : O_Y1);
      STAMP(2 + 12 * L);
    }
    {
      LANE();
      const u32 xo = (s1 + 256 * w) * 1024 + 32 * c + 8 * g;
      u32x2 xr[2];
      if (L == 0) { xr[0] = ld2<0>(rx0, xo, 0); xr[1] = ld2<0>(rx0, xo, 512); } else { xr[0] = ld2<16>(rw, xo, O_X2); xr[1] = ld2<16>(rw, xo, O_X2 + 512); }
      u32x2 G, Br, Bi; u32x4 B2; float tc[2][4], ts[2][4];
#pragma unroll
      for (int q = 0; q < 2; ++q) {
        const float a = (float)(((2 * g + q) * (s1 + 256 * (c >> 1))) & 2047) * (1.f / 2048), cc = cosr(a), ss = sinr(a);
        G[q] = (c & 1) ? pk(ss, cc) : pk(cc, -ss);
        const float bb = (float)(((2 * g + q) * c) & 15) * (1.f / 16), cb = cosr(bb), sb = sinr(bb);
        Br[q] = pk(cb, -sb); Bi[q] = pk(sb, cb);
      }
#pragma unroll
      for (int t = 0; t < 2; ++t)
#pragma unroll
        for (int i = 0; i < 4; ++i) { const float a = (float)((16 * t + 4 * g + i) * c) * (1.f / 512); tc[t][i] = cosr(a); ts[t][i] = sinr(a); PIN(tc[t][i]); PIN(ts[t][i]); }
      {
        float cs[4], sn[4];
#pragma unroll
        for (int j = 0; j < 4; ++j) { const float a = (float)(((4 * g + j) * c) & 31) * (1.f / 32); cs[j] = cosr(a); sn[j] = -sinr(a); }
        B2 = u32x4{pk(cs[0], cs[1]), pk(cs[2], cs[3]), pk(sn[0], sn[1]), pk(sn[2], sn[3])};
      }
      PIN(G); PIN(Br); PIN(Bi); PIN(B2); SB();
      u32x2 y[2];
      { const u32 yo = (s1 * 256 + c + 32 * w) * 32 + g * 8, ys = L ? O_Y2 : O_Y1; int n = 0;
        do { asm volatile("" ::: "memory"); y[0] = ld2<16>(rw, yo, ys); y[1] = ld2<16>(rw, yo, ys + 512); } while (__any(mx4(u32x4{y[0][0], y[0][1], y[1][0], y[1][1]}) == SENT) && ++n < cap);
        if (n >= cap) cap = 0; }
      if (L == 0) { LDGU(1) LDWD(0) LDWD(1) }
      STAMP(3 + 12 * L);
      u32x2* Lp = reinterpret_cast<u32x2*>(scr);
#pragma unroll
      for (int u = 0; u < 2; ++u) { const f32x4 o = mma16(G, y[u], Z4); Lp[g * 264 + c + 16 * (2 * w + u)] = u32x2{pk(o[0], o[1]), pk(o[2], o[3])}; }
      asm volatile("s_waitcnt lgkmcnt(0)" ::: "memory"); __builtin_amdgcn_s_barrier(); asm volatile("" ::: "memory");
      u32 r[2][2];
#pragma unroll
      for (int t = 0; t < 2; ++t)
#pragma unroll
        for (int q = 0; q < 2; ++q) r[t][q] = reinterpret_cast<const u32*>(scr)[((w >> 1) * 264 + c + 16 * t + 32 * (2 * g + q)) * 2 + (w & 1)];
      const float nyq = blo(__builtin_amdgcn_readfirstlane(r[0][0]));
      if (l == 0) r[0][0] = 0u;
      f32x4 acc[2];
#pragma unroll
      for (int t = 0; t < 2; ++t) acc[t] = f32x4{blo(xr[t][0]), bhi(xr[t][0]), blo(xr[t][1]), bhi(xr[t][1])};
#pragma unroll
      for (int t = 0; t < 2; ++t) {
        const u32x2 a = {r[t][0], r[t][1]};
        f32x4 dr = mma16(a, Br, Z4), di = mma16(a, Bi, Z4), re, im;
        if (t == 0) dr[0] += g == 0 ? ((c & 1) ? -nyq : nyq) : 0.f;
#pragma unroll
        for (int i = 0; i < 4; ++i) { re[i] = dr[i] * tc[t][i] - di[i] * ts[t][i]; im[i] = di[i] * tc[t][i] + dr[i] * ts[t][i]; }
        const u32x4 a2 = packc(re, im);
        const u32 f = (t && (c & 1)) ? SGN_BOTH : 0u;
        acc[0] = mma32(a2, B2 ^ f, acc[0]); acc[1] = mma32(a2, B2 ^ (f ^ SGN_HI), acc[1]);
      }
      const u32x4 xq = packc(acc[0], acc[1]);
      st2<16>(u32x2{xq[0], xq[1]}, rw, xo, O_X1 + L * 2 * MB); st2<16>(u32x2{xq[2], xq[3]}, rw, xo, O_X1 + L * 2 * MB + 512);
      float v[8], s = 0.f;
#pragma unroll
      for (int p = 0; p < 4; ++p) { v[2 * p] = blo(xq[p]); v[2 * p + 1] = bhi(xq[p]); s += v[2 * p] + v[2 * p + 1]; }
      const float mean = wave_sum(s) * (1.f / 512);
      float q = 0.f;
#pragma unroll
      for (int j = 0; j < 8; ++j) { v[j] -= mean; q += v[j] * v[j]; }
      const float rs = __builtin_amdgcn_rsqf(wave_sum(q) * (1.f / 512) + 1e-5f);
      u32x4* Xt = reinterpret_cast<u32x4*>(scr + 8448);
      Xt[l * 8 + (w ^ (l & 7))] = u32x4{pk(v[0] * rs, v[1] * rs), pk(v[2] * rs, v[3] * rs), pk(v[4] * rs, v[5] * rs), pk(v[6] * rs, v[7] * rs)};
      asm volatile("s_waitcnt lgkmcnt(0)" ::: "memory"); __builtin_amdgcn_s_barrier(); asm volatile("" ::: "memory");
      st4<16>(Xt[tid ^ ((tid >> 3) & 7)], rw, ((s1 >> 2) * 2048 + (tid >> 3) * 32 + (s1 & 3) * 8 + (tid & 7)) * 16, O_XN + L * 2 * MB);
      STAMP(4 + 12 * L);
    }
    gu32* cnt = (gu32*)(ws + O_CNT + (L * 16 + grp) * 256);
    arrive(cnt);
    LANE();
    if (L == 0) { LDGU(2) LDWD(2) LDWD(3) DRAIN(); }
    if (r_ < 29) {
      await(cnt, 32, cap, w);
      STAMP(5 + 12 * L);
      const u32 xo = (grp * 8 + w) * 32768 + l * 16, xs = O_XN + L * 2 * MB;
      u32x4 xf[32], wf[2][3];
#pragma unroll
      for (int i = 0; i < XW; ++i) xf[i] = ld4<16>(rw, xo, xs + ((i + rot) & 31) * 1024);
#pragma unroll
      for (int i = 0; i < 3; ++i) wf[0][i] = SM[i * 64 + l];
      f32x16 acc[3] = {};
      SB();
#pragma unroll
      for (int ks = 0; ks < 32; ++ks) {
        if (ks + XW < 32) xf[ks + XW] = ld4<16>(rw, xo, xs + ((ks + XW + rot) & 31) * 1024);
        if (ks + 1 < 32) {
#pragma unroll
          for (int i = 0; i < 3; ++i) wf[(ks + 1) & 1][i] = SM[((ks + 1) * 3 + i) * 64 + l];
        }
#pragma unroll
        for (int i = 0; i < 3; ++i) acc[i] = mmb(wf[ks & 1][i], xf[ks], acc[i]);
        SB();
      }
#pragma unroll
      for (int i = 0; i < 3; ++i) {
        float h[8];
#pragma unroll
        for (int r = 0; r < 8; ++r) h[r] = fmaxf(acc[i][r], 0.f) * acc[i][r + 8];
        if (F0 + i < NF) st4<16>(u32x4{pk(h[0], h[1]), pk(h[2], h[3]), pk(h[4], h[5]), pk(h[6], h[7])}, rw, ((grp * 8 + w) * NF + F0 + i) * 1024 + l * 16, O_H + L * 6 * MB);
      }
      STAMP(6 + 12 * L);
      arrive(cnt + 8 * 64);
    }
    if (L == 0) { LDWD(4) LDWD(5) LDWD(6) LDWD(7) LDWD(8) LDWD(9) LDWD(10) }
    {
      await(cnt + 8 * 64, 29, cap, w);
      STAMP(7 + 12 * L);
      const u32 hs = O_H + L * 6 * MB + 2 * TG * NF * 1024;
      const int tau = (2 * TG + (w >> 2)) * 32 + (l & 31);
      const u32 oo = ((tau >> 3) + 256 * (tau & 7)) * 1024 + ((2 * NG + ((w >> 1) & 1)) * 32 + 16 * (l >> 5) + 8 * (w & 1)) * 2;
      u32x4 hf[11][2];
#define LDH(s) { const u32 o_ = hs + min(FSTEP(s), NF - 1) * 1024; hf[s][0] = ld4<16>(rw, l * 16, o_); hf[s][1] = ld4<16>(rw, l * 16, o_ + NF * 1024); }
#pragma unroll
      for (int s = 0; s < HW; ++s) LDH(s)
      f32x16 acc[4] = {};
      u32x4 res;
      SB();
#pragma unroll
      for (int s = 0; s < 11; ++s) {
        if (s + HW < 11) LDH(s + HW)
        if (s + HW == 11) res = ld4<16>(rw, oo, O_X1 + L * 2 * MB);
#pragma unroll
        for (int q = 0; q < 4; ++q) acc[q] = mmb(wd[s][q & 1], hf[s][q >> 1], acc[q]);
        SB();
      }
      STAMP(8 + 12 * L);
      u32x4* Rd = reinterpret_cast<u32x4*>(scr);
      float own[8];
#pragma unroll
      for (int d = 0; d < 8; ++d) {
        const f32x16 A = acc[d >> 1]; const int o = 8 * (d & 1);
        if (d == w) {
#pragma unroll
          for (int i = 0; i < 8; ++i) own[i] = A[o + i];
        } else Rd[(d * 8 + w) * 64 + l] = u32x4{pk(A[o], A[o + 1]), pk(A[o + 2], A[o + 3]), pk(A[o + 4], A[o + 5]), pk(A[o + 6], A[o + 7])};
      }
      __syncthreads();
#pragma unroll
      for (int j = 1; j < 8; ++j) {
        const u32x4 p = Rd[(w * 8 + ((w + j) & 7)) * 64 + l];
#pragma unroll
        for (int i = 0; i < 4; ++i) { own[2 * i] += blo(p[i]); own[2 * i + 1] += bhi(p[i]); }
      }
      u32x4 o;
#pragma unroll
      for (int i = 0; i < 4; ++i) o[i] = pk(own[2 * i] + blo(res[i]), own[2 * i + 1] + bhi(res[i]));
      if (L == 1) { st4<0>(o, ro, oo, 0); STAMP(21); VW(); STAMP(22); FLUSH(0x7FFFFF); break; }
      st4<16>(u32x4{san(o[0]), san(o[1]), san(o[2]), san(o[3])}, rw, oo, O_X2);
      __syncthreads();
      STAMP(9);
    }
    { LANE(); phase_a1<true>(rw, O_X2, rw, O_Z2, scr, s1, w, l, cap); STAMP(11); }
  }
}

extern "C" void kernel_launch(void* const* d_in, const int* in_sizes, int n_in, void* d_out, int out_size,
                              void* d_ws, size_t ws_size, hipStream_t stream) {
  (void)hipFuncSetAttribute((const void*)k_main, hipFuncAttributeMaxDynamicSharedMemorySize, 163840);
  k_pre<<<256 + 784, 512, 0, stream>>>((const u16*)d_in[0], (const u16*)d_in[1], (const u16*)d_in[2], (const u16*)d_in[4], (const u16*)d_in[5],
                                       (const u16*)d_in[6], (char*)d_ws DPASS(0));
  k_main<<<256, 512, 163840, stream>>>((const u16*)d_in[0], (char*)d_ws, (u16*)d_out DPASS(1));
}
```

```cpp
#include <hip/hip_runtime.h>
#include <hip/hip_bf16.h>

typedef unsigned short u16;
typedef unsigned int u32;
typedef float f32x2 __attribute__((ext_vector_type(2)));
typedef float f32x4 __attribute__((ext_vector_type(4)));
typedef float f32x16 __attribute__((ext_vector_type(16)));
typedef u32 u32x2 __attribute__((ext_vector_type(2)));
typedef u32 u32x4 __attribute__((ext_vector_type(4)));
typedef __bf16 bf2 __attribute__((ext_vector_type(2)));
typedef __bf16 bf8 __attribute__((ext_vector_type(8)));
typedef short s16x4 __attribute__((ext_vector_type(4)));

#define DEV __device__ __forceinline__
#define DARG
#define DPASS(n)
#define STAMP(i)
#define FLUSH(m)
#define SARG
#define SPASS
#define VW()
constexpr int NF = 86;
constexpr u32 SGN_HI = 0x80000000u, SGN_LO = 0x00008000u, SGN_BOTH = 0x80008000u;

DEV u32 pk(float a, float b) { f32x2 v = {a, b}; return __builtin_bit_cast(u32, __builtin_convertvector(v, bf2)); }
DEV float blo(u32 v) { return __builtin_bit_cast(float, v << 16); }
DEV float bhi(u32 v) { return __builtin_bit_cast(float, v & 0xffff0000u); }
DEV float cosr(float r) { return __builtin_amdgcn_cosf(r); }
DEV float sinr(float r) { return __builtin_amdgcn_sinf(r); }
DEV f32x4 mma32(u32x4 a, u32x4 b, f32x4 c) { return __builtin_amdgcn_mfma_f32_16x16x32_bf16(__builtin_bit_cast(bf8, a), __builtin_bit_cast(bf8, b), c, 0, 0, 0); }
DEV f32x4 mma16(u32x2 a, u32x2 b, f32x4 c) { return __builtin_amdgcn_mfma_f32_16x16x16bf16_1k(__builtin_bit_cast(s16x4, a), __builtin_bit_cast(s16x4, b), c, 0, 0, 0); }
DEV f32x16 mmb(u32x4 a, u32x4 b, f32x16 c) { return __builtin_amdgcn_mfma_f32_32x32x16_bf16(__builtin_bit_cast(bf8, a), __builtin_bit_cast(bf8, b), c, 0, 0, 0); }
template <int CTRL> DEV float dpp(float v) { return __builtin_bit_cast(float, __builtin_amdgcn_update_dpp(0, __builtin_bit_cast(int, v), CTRL, 0xf, 0xf, true)); }
DEV float wave_sum(float v) {
  v += dpp<0xB1>(v); v += dpp<0x4E>(v); v += dpp<0x141>(v); v += dpp<0x140>(v);
  u32 u = __builtin_bit_cast(u32, v);
  auto r = __builtin_amdgcn_permlane16_swap(u, u, false, false);
  const u32 r0 = r[0], r1 = r[1];
  v = __builtin_bit_cast(float, r0) + __builtin_bit_cast(float, r1);
  u = __builtin_bit_cast(u32, v);
  auto q = __builtin_amdgcn_permlane32_swap(u, u, false, false);
  const u32 q0 = q[0], q1 = q[1];
  return __builtin_bit_cast(float, q0) + __builtin_bit_cast(float, q1);
}
DEV u32x4 packc(const f32x4& re, const f32x4& im) { return u32x4{pk(re[0], re[1]), pk(re[2], re[3]), pk(im[0], im[1]), pk(im[2], im[3])}; }
const f32x4 __device__ Z4 = {0.f, 0.f, 0.f, 0.f};

struct Item { const u16* src; long A; int B, C; float sc; u32x4* dst; };
DEV Item pre_item(int it, int l, const u16* wr, const u16* wi, const u16* wg, const u16* wu, const u16* wd, u32x4* WT, u32x4* WG, u32x4* WD) {
  if (it < 2048) {
    const int pb = it >> 2, r = pb >> 3, m2 = r >> 3, g = (r >> 1) & 3, c = 8 * (r & 1) + (l & 7);
    const int k = 32 * (pb & 7) + 8 * (it & 3) + (l >> 3);
    return {wr + (m2 + 8 * (c + 64 * g)) * 257 + (k ? k : 256), wi - wr, 128 * 257, 256 * 257, k ? 0x1p-19f : 0x1p-20f, WT + (k * 8 + m2) * 64 + 16 * g + c};
  }
  if (it < 2048 + 2752) {
    const int q = it - 2048, F = q >> 5, r = l & 31, lam = 2 * (q & 31) + (l >> 5), d0 = 16 * (lam & 15) + 4 * (lam >> 4);
    return {(r < 16 ? wg : wu) + 16 * F + (r & 15) + d0 * 1376, 1376, 2752, 256 * 1376, 1.f, WG + q * 64 + l};
  }
  const int q = min(it - 4800, 1407), nt = q & 15, F = q >> 4, r = l & 31;
  const int n = 32 * nt + 16 * ((r >> 2) & 1) + (r & 3) + 4 * (r >> 3);
  return {wd + (16 * min(F, NF - 1) + 4 * (l >> 5)) * 512 + n, 512, 1024, 4096, F < NF ? 1.f : 0.f, WD + (nt * 88 + F) * 64 + l};
}

constexpr u32 MB = 1u << 20;
constexpr u32 O_Z1 = 0, O_Y1 = 2 * MB, O_Z2 = 4 * MB, O_Y2 = 6 * MB, O_X2 = 8 * MB, O_WT = 10 * MB, O_X1 = 12 * MB  ,
              O_XN = 16 * MB  , O_WG = 20 * MB, O_WD = 23 * MB, O_CNT = 25 * MB, O_H = 26 * MB  , WS_END = 38 * MB;
constexpr u32 SENT = 0xFFFFFFFFu;
constexpr int SPIN_CAP = 1 << 18;
typedef __amdgpu_buffer_rsrc_t rsrc_t;
typedef __attribute__((address_space(1))) u32 gu32;
DEV rsrc_t mkrs(const void* p, u32 n) { return __builtin_amdgcn_make_buffer_rsrc(const_cast<void*>(p), 0, n, 0x00020000); }
template <int AUX> DEV u32x4 ld4(rsrc_t r, u32 v, u32 s) { return __builtin_amdgcn_raw_buffer_load_b128(r, v, s, AUX); }
template <int AUX> DEV u32x2 ld2(rsrc_t r, u32 v, u32 s) { return __builtin_amdgcn_raw_buffer_load_b64(r, v, s, AUX); }
template <int AUX> DEV void st4(u32x4 d, rsrc_t r, u32 v, u32 s) { __builtin_amdgcn_raw_buffer_store_b128(d, r, v, s, AUX); }
template <int AUX> DEV void st2(u32x2 d, rsrc_t r, u32 v, u32 s) { __builtin_amdgcn_raw_buffer_store_b64(d, r, v, s, AUX); }
DEV u32x4 swp(u32x4 v) {
  auto p = __builtin_amdgcn_permlane16_swap(v[0], v[2], false, false); const u32 p0 = p[0], p1 = p[1];
  auto q = __builtin_amdgcn_permlane16_swap(v[1], v[3], false, false); const u32 q0 = q[0], q1 = q[1];
  return u32x4{p0, q0, p1, q1};
}
#define PSL 0
DEV u32 san(u32 v) { return min(v, SENT - 1u); }
DEV u32 mx4(u32x4 v) { return max(max(v[0], v[1]), max(v[2], v[3])); }
#define PIN(x) asm volatile("" : "+v"(x))
#define LANE() int l = tid0 & 63, w = w0, rot = rot0, rot2 = rot20; PIN(l); asm volatile("" : "+s"(w), "+s"(rot), "+s"(rot2)); const int c = l & 15, g = l >> 4, tid = w * 64 + l; (void)c; (void)g; (void)tid
#define SB() __builtin_amdgcn_sched_barrier(0)
#define DRAIN() asm volatile("s_waitcnt vmcnt(0)" ::: "memory")
DEV u32x4 poll4(rsrc_t r, u32 v, u32 s, int& cap) {
  u32x4 d; int n = 0;
  for (;;) { asm volatile("" ::: "memory"); d = ld4<16>(r, v, s); if (!__any(mx4(d) == SENT) || ++n >= cap) break; if (PSL) __builtin_amdgcn_s_sleep(PSL); }
  if (n >= cap) cap = 0;
  return d;
}
#define NSH 32
DEV void arrive(gu32* c) { DRAIN(); __syncthreads(); if (threadIdx.x == 0) __hip_atomic_fetch_add(c, 1u, __ATOMIC_RELAXED, __HIP_MEMORY_SCOPE_AGENT); }
DEV void await(gu32* c, int last, int& cap, int w, int l) {
  if (w == 0) {
    const int i = l & (NSH - 1); const u32 n = (last - i + NSH) / NSH; int k = 0;
    while (__any(__hip_atomic_load(c + i * 64, __ATOMIC_RELAXED, __HIP_MEMORY_SCOPE_AGENT) < n) && ++k < cap) __builtin_amdgcn_s_sleep(1);
    if (k >= cap) cap = 0;
  }
  __syncthreads();
}

template <bool POLL> DEV void phase_a1(rsrc_t rx, u32 xs, rsrc_t rw, u32 zs, char* scr, int s1, int w, int l, int& cap SARG) {
  const int c = l & 15, g = l >> 4;
  const u32 xo = (s1 + 256 * w) * 1024 + l * 16;
  u32x4 xd;
  if (!POLL) xd = ld4<0>(rx, xo, xs);
  u32x4 Bc, Bs, Am; u32x2 G; float tc[2][4], ts[2][4];
#pragma unroll
  for (int p = 0; p < 4; ++p) {
    const float a0 = (float)(((8 * g + 2 * p) * c) & 31) * (1.f / 32), a1 = (float)(((8 * g + 2 * p + 1) * c) & 31) * (1.f / 32);
    Bc[p] = pk(cosr(a0), cosr(a1)); Bs[p] = pk(-sinr(a0), -sinr(a1));
  }
#pragma unroll
  for (int t = 0; t < 2; ++t)
#pragma unroll
    for (int i = 0; i < 4; ++i) { const float a = (float)((c + 16 * t) * (4 * g + i)) * (1.f / 512); tc[t][i] = cosr(a); ts[t][i] = sinr(a); PIN(tc[t][i]); PIN(ts[t][i]); }
  {
    float cs[4], sn[4];
#pragma unroll
    for (int j = 0; j < 4; ++j) { const float a = (float)(((4 * g + j) * (c >> 1)) & 15) * (1.f / 16); cs[j] = cosr(a); sn[j] = sinr(a); }
    const u32 c0 = pk(cs[0], cs[1]), c1 = pk(cs[2], cs[3]), s0 = pk(sn[0], sn[1]), s1_ = pk(sn[2], sn[3]);
    Am = (c & 1) ? u32x4{s0 ^ SGN_BOTH, s1_ ^ SGN_BOTH, c0, c1} : u32x4{c0, c1, s0, s1_};
#pragma unroll
    for (int q = 0; q < 2; ++q) {
      const float a = (float)(((c >> 1) * (s1 + 256 * (2 * g + q))) & 2047) * (1.f / 2048), cc = cosr(a), ss = sinr(a);
      G[q] = (c & 1) ? pk(-ss, cc) : pk(cc, ss);
    }
  }
  PIN(Bc); PIN(Bs); PIN(Am); PIN(G); SB();
  if (POLL) { xd = poll4(rx, xo, xs, cap); STAMP(10); }
  char* tr = scr + 16384 + w * 1280;
  *reinterpret_cast<u32x4*>(tr + g * 288 + c * 16) = xd;
  float v[8], s = 0.f;
#pragma unroll
  for (int p = 0; p < 4; ++p) s += blo(xd[p]) + bhi(xd[p]);
  __builtin_amdgcn_fence(__ATOMIC_RELEASE, "wavefront"); __builtin_amdgcn_wave_barrier(); __builtin_amdgcn_fence(__ATOMIC_ACQUIRE, "wavefront");
#pragma unroll
  for (int j = 0; j < 8; ++j) v[j] = blo(*reinterpret_cast<const u16*>(tr + g * 288 + j * 32 + c * 2));
  const float mean = wave_sum(s) * (1.f / 512);
  float q = 0.f;
#pragma unroll
  for (int j = 0; j < 8; ++j) { v[j] -= mean; q += v[j] * v[j]; }
  const float rs = __builtin_amdgcn_rsqf(wave_sum(q) * (1.f / 512) + 1e-5f);
  const u32x4 a = {pk(v[0] * rs, v[1] * rs), pk(v[2] * rs, v[3] * rs), pk(v[4] * rs, v[5] * rs), pk(v[6] * rs, v[7] * rs)};
  const u32 ny = c == 0 ? 0xBF803F80u : 0u;
  u32* L = reinterpret_cast<u32*>(scr);
#pragma unroll
  for (int t = 0; t < 2; ++t) {
    const u32 f = t ? SGN_HI : 0u;
    const f32x4 dr = mma32(a, Bc ^ f, Z4), di = mma32(a, Bs ^ f, Z4);
    f32x4 re, im;
#pragma unroll
    for (int i = 0; i < 4; ++i) { re[i] = dr[i] * tc[t][i] + di[i] * ts[t][i]; im[i] = di[i] * tc[t][i] - dr[i] * ts[t][i]; }
    const u32x4 b = packc(re, im);
    f32x4 R = mma32(Am, b, Z4);
    if (t == 0) { const f32x4 n = mma32(u32x4{ny, ny, 0u, 0u}, b, Z4); if (l == 0) { R[0] = n[0]; R[1] = 0.f; } }
#pragma unroll
    for (int q2 = 0; q2 < 2; ++q2) L[((w >> 1) * 264 + c + 16 * t + 32 * (2 * g + q2)) * 2 + (w & 1)] = pk(R[2 * q2], R[2 * q2 + 1]);
  }
  __syncthreads();
  const f32x4 o0 = mma16(G, reinterpret_cast<const u32x2*>(L)[g * 264 + c + 32 * w], Z4), o1 = mma16(G, reinterpret_cast<const u32x2*>(L)[g * 264 + c + 32 * w + 16], Z4);
  st4<POLL ? 16 : 0>(swp(u32x4{san(pk(o0[0], o0[1])), san(pk(o0[2], o0[3])), san(pk(o1[0], o1[1])), san(pk(o1[2], o1[3]))}), rw,
                     ((c + 32 * w + 16 * (g & 1)) * 256 + s1) * 32 + (g & 2) * 8, zs);
}

__global__ __launch_bounds__(512, 2) void k_pre(const u16* __restrict__ x, const u16* __restrict__ wr, const u16* __restrict__ wi, const u16* __restrict__ wg,
                                                const u16* __restrict__ wu, const u16* __restrict__ wd, char* __restrict__ ws DARG) {
  __shared__ __attribute__((aligned(16))) char scr[16384 + 8 * 1280 + 256];
  u32 stv = 0; (void)stv;
  const int b = blockIdx.x, tid = threadIdx.x;
  if (b >= 256) {
    const int it = (b - 256) * 8 + __builtin_amdgcn_readfirstlane(tid >> 6);
    u32 v[3][8]; float sc[3]; u32x4* dst[3];
#pragma unroll
    for (int q = 0; q < 3; ++q) {
      const Item I = pre_item(it + 2080 * q, tid & 63, wr, wi, wg, wu, wd, (u32x4*)(ws + O_WT), (u32x4*)(ws + O_WG), (u32x4*)(ws + O_WD));
      sc[q] = I.sc; dst[q] = I.dst;
#pragma unroll
      for (int j = 0; j < 8; ++j) v[q][j] = I.src[(j & 1) * I.A + ((j >> 1) & 1) * I.B + (j >> 2) * I.C];
    }
#pragma unroll
    for (int q = 0; q < 3; ++q)
      *dst[q] = u32x4{pk(blo(v[q][0]) * sc[q], blo(v[q][1]) * sc[q]), pk(blo(v[q][2]) * sc[q], blo(v[q][3]) * sc[q]),
                      pk(blo(v[q][4]) * sc[q], blo(v[q][5]) * sc[q]), pk(blo(v[q][6]) * sc[q], blo(v[q][7]) * sc[q])};
    return;
  }
  STAMP(0);
  const rsrc_t rw = mkrs(ws, WS_END), rx = mkrs(x, 2 * MB);
  if (tid < 4) *reinterpret_cast<u32*>(ws + O_CNT + (b * 4 + tid) * 256) = 0u;
  int cap = 0;
  phase_a1<false>(rx, 0, rw, O_Z1, scr, (b & 7) * 32 + (b >> 3), __builtin_amdgcn_readfirstlane(tid >> 6), tid & 63, cap SPASS);
  st4<0>(u32x4{SENT, SENT, SENT, SENT}, rw, (b * 512 + tid) * 16, O_Y1);
  STAMP(1); VW(); STAMP(4); FLUSH(19);
}

DEV void glds16(const void* gsrc, u32 lds_dst) {
  u32 keep;
  asm volatile("s_mov_b32 %0, m0\n\ts_mov_b32 m0, %2\n\ts_nop 0\n\tglobal_load_lds_dwordx4 %1, off\n\ts_mov_b32 m0, %0" : "=&s"(keep) : "v"(gsrc), "s"(lds_dst) : "memory");
}
#define XW 10
#define XA 16
#define PRIO 2
constexpr int HW = 5;

__global__ __launch_bounds__(512, 2) void k_main(const u16* __restrict__ x, char* __restrict__ ws, u16* __restrict__ out DARG) {
  extern __shared__ __attribute__((aligned(16))) u32x4 SM[];
  char* scr = reinterpret_cast<char*>(SM) + 98304;
  u32 stv = 0; (void)stv;
  const int b = blockIdx.x, tid0 = threadIdx.x, w0 = __builtin_amdgcn_readfirstlane(tid0 >> 6);
  const int grp = b & 7, r_ = b >> 3, s1 = grp * 32 + r_;
  const rsrc_t rw = mkrs(ws, WS_END), rx0 = mkrs(x, 2 * MB), ro = mkrs(out, 2 * MB);
  int cap = SPIN_CAP;
  STAMP(0);
  const u32x4 wt = ld4<0>(rw, tid0 * 16, O_WT + s1 * 8192);
  const int NG = r_ & 7, TG = grp * 4 + (r_ >> 3), F0 = r_ * 3, rot0 = (r_ * 9) & 31, rot20 = (NG * 11) >> 3;
  u32x4 wd[11][2];
#define FSTEP(s) (11 * w + (s) + rot2 - ((s) + rot2 >= 11 ? 11 : 0))
#define LDWD(s) { const u32 o_ = O_WD + (2 * NG * 88 + FSTEP(s)) * 1024; wd[s][0] = ld4<0>(rw, l * 16, o_); wd[s][1] = ld4<0>(rw, l * 16, o_ + 88 * 1024); }
  const u32 lb = (u32)(size_t)(__attribute__((address_space(3))) u32x4*)SM;
#define LDGU(i) { const char* src = ws + O_WG + (min(F0 + (i), NF - 1) * 32 + w * 4) * 1024 + l * 16; \
    _Pragma("unroll") for (int j = 0; j < 4; ++j) glds16(src + j * 1024, lb + (((4 * w + j - rot) & 31) * 3 + (i)) * 1024); }

  for (int L = 0; L < 2; ++L) {
    {
      LANE();
      u32x4 zd;
      if (L == 0) zd = ld4<0>(rw, tid * 16, O_Z1 + s1 * 8192);
      float cs[4], sn[4], tc[4], ts[4];
#pragma unroll
      for (int j = 0; j < 4; ++j) {
        const int p = (4 * g + j) * c;
        const float a = (float)(p & 15) * (1.f / 16), bb = (float)p * (1.f / 256);
        cs[j] = cosr(a); sn[j] = sinr(a); tc[j] = cosr(bb); ts[j] = sinr(bb); PIN(tc[j]); PIN(ts[j]);
      }
      u32x4 B1r = {pk(cs[0], sn[0]), pk(cs[1], sn[1]), pk(cs[2], sn[2]), pk(cs[3], sn[3])};
      u32x4 B1i = {pk(-sn[0], cs[0]), pk(-sn[1], cs[1]), pk(-sn[2], cs[2]), pk(-sn[3], cs[3])};
      u32x4 Fr = {pk(cs[0], cs[1]), pk(cs[2], cs[3]), pk(sn[0], sn[1]), pk(sn[2], sn[3])};
      PIN(B1r); PIN(B1i); PIN(Fr); SB();
      if (L == 1) zd = poll4(rw, tid * 16, O_Z2 + s1 * 8192, cap);
      u32* P = reinterpret_cast<u32*>(scr + 8448);
#pragma unroll
      for (int i = 0; i < 4; ++i) { const int sa = tid >> 1; P[(4 * (tid & 1) + i) * 312 + sa + 16 * (sa >> 6)] = zd[i]; }
      if (L == 0) st4<16>(u32x4{SENT, SENT, SENT, SENT}, rw, tid * 16, O_Z2 + s1 * 8192);
      if (L == 0) LDGU(0)
      asm volatile("s_waitcnt lgkmcnt(0)" ::: "memory"); __builtin_amdgcn_s_barrier(); asm volatile("" ::: "memory");
      STAMP(1 + 12 * L);
      u32x4 a;
#pragma unroll
      for (int j = 0; j < 4; ++j) a[j] = P[w * 312 + 80 * g + 16 * j + c];
      const u32x4 Fi = {Fr[2] ^ SGN_BOTH, Fr[3] ^ SGN_BOTH, Fr[0], Fr[1]}, Ir = {Fr[0], Fr[1], Fi[0], Fi[1]}, Ii = {Fr[2], Fr[3], Fr[0], Fr[1]};
      f32x4 dr = mma32(a, B1r, Z4), di = mma32(a, B1i, Z4), re, im;
#pragma unroll
      for (int i = 0; i < 4; ++i) { re[i] = dr[i] * tc[i] + di[i] * ts[i]; im[i] = di[i] * tc[i] - dr[i] * ts[i]; }
      u32x4 bb = packc(re, im);
      dr = mma32(Fr, bb, Z4); di = mma32(Fi, bb, Z4);
#pragma unroll
      for (int i = 0; i < 4; ++i) { const float wr = blo(wt[i]), wi = bhi(wt[i]); re[i] = dr[i] * wr - di[i] * wi; im[i] = di[i] * wr + dr[i] * wi; }
      bb = packc(re, im);
      dr = mma32(bb, Ir, Z4); di = mma32(bb, Ii, Z4);
#pragma unroll
      for (int i = 0; i < 4; ++i) { re[i] = dr[i] * tc[i] - di[i] * ts[i]; im[i] = di[i] * tc[i] + dr[i] * ts[i]; }
      bb = packc(re, im);
      dr = mma32(Ir, bb, Z4); di = mma32(Ii, bb, Z4);
      u32* Q = reinterpret_cast<u32*>(scr + 20480);
#pragma unroll
      for (int i = 0; i < 4; ++i) Q[(16 * (4 * g + i) + c) * 8 + (w ^ (((c >> 2) & 1) * 4))] = san(pk(dr[i], di[i]));
      asm volatile("s_waitcnt vmcnt(4) lgkmcnt(0)" ::: "memory"); __builtin_amdgcn_s_barrier(); asm volatile("" ::: "memory");
      st4<16>(reinterpret_cast<const u32x4*>(Q)[tid ^ ((tid >> 3) & 1)], rw, ((tid >> 1) * 256 + s1) * 32 + (tid & 1) * 16, L ? O_Y2 : O_Y1);
      STAMP(2 + 12 * L);
    }
    {
      LANE();
      const u32 xo = (s1 + 256 * w) * 1024 + 32 * c + 8 * (g & 2) + 512 * (g & 1);
      u32x4 xr;
      if (L == 0) xr = ld4<0>(rx0, xo, 0); else xr = ld4<16>(rw, xo, O_X2);
      u32x2 G, Br, Bi; u32x4 B2; float tc[2][4], ts[2][4];
#pragma unroll
      for (int q = 0; q < 2; ++q) {
        const float a = (float)(((2 * g + q) * (s1 + 256 * (c >> 1))) & 2047) * (1.f / 2048), cc = cosr(a), ss = sinr(a);
        G[q] = (c & 1) ? pk(ss, cc) : pk(cc, -ss);
        const float bb = (float)(((2 * g + q) * c) & 15) * (1.f / 16), cb = cosr(bb), sb = sinr(bb);
        Br[q] = pk(cb, -sb); Bi[q] = pk(sb, cb);
      }
#pragma unroll
      for (int t = 0; t < 2; ++t)
#pragma unroll
        for (int i = 0; i < 4; ++i) { const float a = (float)((16 * t + 4 * g + i) * c) * (1.f / 512); tc[t][i] = cosr(a); ts[t][i] = sinr(a); PIN(tc[t][i]); PIN(ts[t][i]); }
      {
        float cs[4], sn[4];
#pragma unroll
        for (int j = 0; j < 4; ++j) { const float a = (float)(((4 * g + j) * c) & 31) * (1.f / 32); cs[j] = cosr(a); sn[j] = -sinr(a); }
        B2 = u32x4{pk(cs[0], cs[1]), pk(cs[2], cs[3]), pk(sn[0], sn[1]), pk(sn[2], sn[3])};
      }
      PIN(G); PIN(Br); PIN(Bi); PIN(B2); SB();
      const u32x4 y = swp(poll4(rw, (s1 * 256 + c + 32 * w + 16 * (g & 1)) * 32 + (g & 2) * 8, L ? O_Y2 : O_Y1, cap));
      if (L == 0) {
        st4<16>(u32x4{SENT, SENT, SENT, SENT}, rw, tid * 16, O_Y2 + s1 * 8192);
        st4<16>(u32x4{SENT, SENT, SENT, SENT}, rw, l * 16, O_X2 + (s1 + 256 * w) * 1024);
        LDGU(1) LDWD(0) LDWD(1)
      }
      STAMP(3 + 12 * L);
      u32x2* Lp = reinterpret_cast<u32x2*>(scr);
#pragma unroll
      for (int u = 0; u < 2; ++u) { const f32x4 o = mma16(G, u32x2{y[2 * u], y[2 * u + 1]}, Z4); Lp[g * 264 + c + 16 * (2 * w + u)] = u32x2{pk(o[0], o[1]), pk(o[2], o[3])}; }
      asm volatile("s_waitcnt lgkmcnt(0)" ::: "memory"); __builtin_amdgcn_s_barrier(); asm volatile("" ::: "memory");
      u32 r[2][2];
#pragma unroll
      for (int t = 0; t < 2; ++t)
#pragma unroll
        for (int q = 0; q < 2; ++q) r[t][q] = reinterpret_cast<const u32*>(scr)[((w >> 1) * 264 + c + 16 * t + 32 * (2 * g + q)) * 2 + (w & 1)];
      const float nyq = blo(__builtin_amdgcn_readfirstlane(r[0][0]));
      if (l == 0) r[0][0] = 0u;
      f32x4 acc[2];
      xr = swp(xr);
#pragma unroll
      for (int t = 0; t < 2; ++t) acc[t] = f32x4{blo(xr[2 * t]), bhi(xr[2 * t]), blo(xr[2 * t + 1]), bhi(xr[2 * t + 1])};
#pragma unroll
      for (int t = 0; t < 2; ++t) {
        const u32x2 a = {r[t][0], r[t][1]};
        f32x4 dr = mma16(a, Br, Z4), di = mma16(a, Bi, Z4), re, im;
        if (t == 0) dr[0] += g == 0 ? ((c & 1) ? -nyq : nyq) : 0.f;
#pragma unroll
        for (int i = 0; i < 4; ++i) { re[i] = dr[i] * tc[t][i] - di[i] * ts[t][i]; im[i] = di[i] * tc[t][i] + dr[i] * ts[t][i]; }
        const u32x4 a2 = packc(re, im);
        const u32 f = (t && (c & 1)) ? SGN_BOTH : 0u;
        acc[0] = mma32(a2, B2 ^ f, acc[0]); acc[1] = mma32(a2, B2 ^ (f ^ SGN_HI), acc[1]);
      }
      const u32x4 xq = packc(acc[0], acc[1]);
      st4<16>(swp(xq), rw, xo, O_X1 + L * 2 * MB);
      float v[8], s = 0.f;
#pragma unroll
      for (int p = 0; p < 4; ++p) { v[2 * p] = blo(xq[p]); v[2 * p + 1] = bhi(xq[p]); s += v[2 * p] + v[2 * p + 1]; }
      const float mean = wave_sum(s) * (1.f / 512);
      float q = 0.f;
#pragma unroll
      for (int j = 0; j < 8; ++j) { v[j] -= mean; q += v[j] * v[j]; }
      const float rs = __builtin_amdgcn_rsqf(wave_sum(q) * (1.f / 512) + 1e-5f);
      u32x4* Xt = reinterpret_cast<u32x4*>(scr + 8448);
      Xt[l * 8 + (w ^ (l & 7))] = u32x4{pk(v[0] * rs, v[1] * rs), pk(v[2] * rs, v[3] * rs), pk(v[4] * rs, v[5] * rs), pk(v[6] * rs, v[7] * rs)};
      asm volatile("s_waitcnt lgkmcnt(0)" ::: "memory"); __builtin_amdgcn_s_barrier(); asm volatile("" ::: "memory");
      st4<16>(Xt[tid ^ ((tid >> 3) & 7)], rw, ((s1 >> 2) * 2048 + (tid >> 3) * 32 + (s1 & 3) * 8 + (tid & 7)) * 16, O_XN + L * 2 * MB);
      STAMP(4 + 12 * L);
    }
    gu32* cnt = (gu32*)(ws + O_CNT + (L * 16 + grp) * 8192);
    arrive(cnt + (r_ & (NSH - 1)) * 64); STAMP(24 + 4 * L);
    LANE();
    if (L == 0) { LDGU(2) LDWD(2) LDWD(3) DRAIN(); }
    if (r_ < 29) {
      await(cnt, 31, cap, w, l);
      STAMP(5 + 12 * L);
      const u32 xo = (grp * 8 + w) * 32768 + l * 16, xs = O_XN + L * 2 * MB;
      u32x4 xf[32], wf[2][3];
#pragma unroll
      for (int i = 0; i < XW; ++i) xf[i] = ld4<XA>(rw, xo, xs + ((i + rot) & 31) * 1024);
#pragma unroll
      for (int i = 0; i < 3; ++i) wf[0][i] = SM[i * 64 + l];
      f32x16 acc[3] = {};
      if (w < 4) __builtin_amdgcn_s_setprio(PRIO);
      SB();
#pragma unroll
      for (int ks = 0; ks < 32; ++ks) {

        if (ks + XW < 32) xf[ks + XW] = ld4<XA>(rw, xo, xs + ((ks + XW + rot) & 31) * 1024);
        if (ks + 1 < 32) {
#pragma unroll
          for (int i = 0; i < 3; ++i) wf[(ks + 1) & 1][i] = SM[((ks + 1) * 3 + i) * 64 + l];
        }
        SB();
#pragma unroll
        for (int i = 0; i < 3; ++i) acc[i] = mmb(wf[ks & 1][i], xf[ks], acc[i]);
        SB();
      }
#pragma unroll
      for (int i = 0; i < 3; ++i) {
        float h[8];
#pragma unroll
        for (int r = 0; r < 8; ++r) h[r] = fmaxf(acc[i][r], 0.f) * acc[i][r + 8];
        if (F0 + i < NF) st4<16>(u32x4{pk(h[0], h[1]), pk(h[2], h[3]), pk(h[4], h[5]), pk(h[6], h[7])}, rw, ((grp * 8 + w) * NF + F0 + i) * 1024 + l * 16, O_H + L * 6 * MB);
      }
      __builtin_amdgcn_s_setprio(0);
      STAMP(6 + 12 * L);
      arrive(cnt + 8 * 2048 + (r_ & (NSH - 1)) * 64); STAMP(25 + 4 * L);
    }
    if (L == 0) { LDWD(4) LDWD(5) LDWD(6) LDWD(7) LDWD(8) LDWD(9) LDWD(10) }
    {
      await(cnt + 8 * 2048, 28, cap, w, l);
      STAMP(7 + 12 * L);
      const u32 hs = O_H + L * 6 * MB + 2 * TG * NF * 1024;
      const u32 oo = (8 * TG + w + 256 * (l >> 3)) * 1024 + NG * 128 + (l & 7) * 16;
      u32x4 hf[11][2];
#define LDH(s) { const u32 o_ = hs + min(FSTEP(s), NF - 1) * 1024; hf[s][0] = ld4<16>(rw, l * 16, o_); hf[s][1] = ld4<16>(rw, l * 16, o_ + NF * 1024); }
#pragma unroll
      for (int s = 0; s < HW; ++s) LDH(s)
      f32x16 acc[4] = {};
      u32x4 res;
      SB();
#pragma unroll
      for (int s = 0; s < 11; ++s) {
        if (s + HW < 11) LDH(s + HW)
        if (s + HW == 11) res = ld4<16>(rw, oo, O_X1 + L * 2 * MB);
        SB();
#pragma unroll
        for (int q = 0; q < 4; ++q) acc[q] = mmb(wd[s][q & 1], hf[s][q >> 1], acc[q]);
        SB();
      }
      STAMP(8 + 12 * L);
      u32x4* Rd = reinterpret_cast<u32x4*>(scr);
#pragma unroll
      for (int d = 0; d < 8; ++d) {
        const f32x16 A = acc[d >> 1]; const int o = 8 * (d & 1);
        Rd[w * 512 + (32 * (d >> 2) + (l & 31)) * 8 + ((4 * ((d >> 1) & 1) + 2 * (l >> 5) + (d & 1)) ^ (l & 7))] =
            u32x4{pk(A[o], A[o + 1]), pk(A[o + 2], A[o + 3]), pk(A[o + 4], A[o + 5]), pk(A[o + 6], A[o + 7])};
      }
      asm volatile("s_waitcnt lgkmcnt(0)" ::: "memory"); __builtin_amdgcn_s_barrier(); asm volatile("" ::: "memory");
      STAMP(26 + 4 * L);
      float own[8] = {};
#pragma unroll
      for (int j = 0; j < 8; ++j) {
        const u32x4 p = Rd[j * 512 + w * 64 + (l ^ (l >> 3))];
#pragma unroll
        for (int i = 0; i < 4; ++i) { own[2 * i] += blo(p[i]); own[2 * i + 1] += bhi(p[i]); }
      }
      u32x4 o;
#pragma unroll
      for (int i = 0; i < 4; ++i) o[i] = pk(own[2 * i] + blo(res[i]), own[2 * i + 1] + bhi(res[i]));
      if (L == 1) { st4<0>(o, ro, oo, 0); STAMP(21); VW(); STAMP(22); FLUSH(r_ < 29 ? 0x77FFEFFFu : 0x557FEFFFu & ~0x60060u); break; }
      st4<16>(u32x4{san(o[0]), san(o[1]), san(o[2]), san(o[3])}, rw, oo, O_X2);
      asm volatile("s_waitcnt lgkmcnt(0)" ::: "memory"); __builtin_amdgcn_s_barrier(); asm volatile("" ::: "memory");
      STAMP(9);
    }
    { LANE(); phase_a1<true>(rw, O_X2, rw, O_Z2, scr, s1, w, l, cap SPASS); STAMP(11); }
  }
}

extern "C" void kernel_launch(void* const* d_in, const int* in_sizes, int n_in, void* d_out, int out_size,
                              void* d_ws, size_t ws_size, hipStream_t stream) {
  (void)hipFuncSetAttribute((const void*)k_main, hipFuncAttributeMaxDynamicSharedMemorySize, 163840);
  k_pre<<<256 + 260, 512, 0, stream>>>((const u16*)d_in[0], (const u16*)d_in[1], (const u16*)d_in[2], (const u16*)d_in[4], (const u16*)d_in[5],
                                       (const u16*)d_in[6], (char*)d_ws DPASS(0));
  k_main<<<256, 512, 163840, stream>>>((const u16*)d_in[0], (char*)d_ws, (u16*)d_out DPASS(1));
}
```

```cpp
#include <hip/hip_runtime.h>
#include <hip/hip_bf16.h>

typedef unsigned short u16;
typedef unsigned int u32;
typedef float f32x2 __attribute__((ext_vector_type(2)));
typedef float f32x4 __attribute__((ext_vector_type(4)));
typedef float f32x16 __attribute__((ext_vector_type(16)));
typedef u32 u32x2 __attribute__((ext_vector_type(2)));
typedef u32 u32x4 __attribute__((ext_vector_type(4)));
typedef __bf16 bf2 __attribute__((ext_vector_type(2)));
typedef __bf16 bf8 __attribute__((ext_vector_type(8)));
typedef short s16x4 __attribute__((ext_vector_type(4)));

#define DEV __device__ __forceinline__
#define DARG
#define DPASS(n)
#define STAMP(i)
#define FLUSH(m)
#define SARG
#define SPASS
#define VW()
constexpr int NF = 86;
constexpr u32 SGN_HI = 0x80000000u, SGN_LO = 0x00008000u, SGN_BOTH = 0x80008000u;

DEV u32 pk(float a, float b) { f32x2 v = {a, b}; return __builtin_bit_cast(u32, __builtin_convertvector(v, bf2)); }
DEV float blo(u32 v) { return __builtin_bit_cast(float, v << 16); }
DEV float bhi(u32 v) { return __builtin_bit_cast(float, v & 0xffff0000u); }
DEV float cosr(float r) { return __builtin_amdgcn_cosf(r); }
DEV float sinr(float r) { return __builtin_amdgcn_sinf(r); }
DEV f32x4 mma32(u32x4 a, u32x4 b, f32x4 c) { return __builtin_amdgcn_mfma_f32_16x16x32_bf16(__builtin_bit_cast(bf8, a), __builtin_bit_cast(bf8, b), c, 0, 0, 0); }
DEV f32x4 mma16(u32x2 a, u32x2 b, f32x4 c) { return __builtin_amdgcn_mfma_f32_16x16x16bf16_1k(__builtin_bit_cast(s16x4, a), __builtin_bit_cast(s16x4, b), c, 0, 0, 0); }
DEV f32x16 mmb(u32x4 a, u32x4 b, f32x16 c) { return __builtin_amdgcn_mfma_f32_32x32x16_bf16(__builtin_bit_cast(bf8, a), __builtin_bit_cast(bf8, b), c, 0, 0, 0); }
template <int CTRL> DEV float dpp(float v) { return __builtin_bit_cast(float, __builtin_amdgcn_update_dpp(0, __builtin_bit_cast(int, v), CTRL, 0xf, 0xf, true)); }
DEV float wave_sum(float v) {
  v += dpp<0xB1>(v); v += dpp<0x4E>(v); v += dpp<0x141>(v); v += dpp<0x140>(v);
  u32 u = __builtin_bit_cast(u32, v);
  auto r = __builtin_amdgcn_permlane16_swap(u, u, false, false);
  const u32 r0 = r[0], r1 = r[1];
  v = __builtin_bit_cast(float, r0) + __builtin_bit_cast(float, r1);
  u = __builtin_bit_cast(u32, v);
  auto q = __builtin_amdgcn_permlane32_swap(u, u, false, false);
  const u32 q0 = q[0], q1 = q[1];
  return __builtin_bit_cast(float, q0) + __builtin_bit_cast(float, q1);
}
DEV u32x4 packc(const f32x4& re, const f32x4& im) { return u32x4{pk(re[0], re[1]), pk(re[2], re[3]), pk(im[0], im[1]), pk(im[2], im[3])}; }
const f32x4 __device__ Z4 = {0.f, 0.f, 0.f, 0.f};

constexpr u32 MB = 1u << 20;
constexpr u32 O_Z1 = 0, O_Y1 = 2 * MB, O_Z2 = 4 * MB, O_Y2 = 6 * MB, O_X2 = 8 * MB, O_WT = 10 * MB, O_X1 = 12 * MB  ,
              O_XN = 16 * MB  , O_WG = 20 * MB, O_WD = 23 * MB, O_CNT = 25 * MB, O_H = 26 * MB  , WS_END = 38 * MB;
constexpr u32 SENT = 0xFFFFFFFFu;
constexpr int SPIN_CAP = 1 << 18;
typedef __amdgpu_buffer_rsrc_t rsrc_t;
typedef __attribute__((address_space(1))) u32 gu32;
DEV rsrc_t mkrs(const void* p, u32 n) { return __builtin_amdgcn_make_buffer_rsrc(const_cast<void*>(p), 0, n, 0x00020000); }
template <int AUX> DEV u32x4 ld4(rsrc_t r, u32 v, u32 s) { return __builtin_amdgcn_raw_buffer_load_b128(r, v, s, AUX); }
template <int AUX> DEV u32x2 ld2(rsrc_t r, u32 v, u32 s) { return __builtin_amdgcn_raw_buffer_load_b64(r, v, s, AUX); }
template <int AUX> DEV void st4(u32x4 d, rsrc_t r, u32 v, u32 s) { __builtin_amdgcn_raw_buffer_store_b128(d, r, v, s, AUX); }
template <int AUX> DEV void st2(u32x2 d, rsrc_t r, u32 v, u32 s) { __builtin_amdgcn_raw_buffer_store_b64(d, r, v, s, AUX); }
DEV u32x4 swp(u32x4 v) {
  auto p = __builtin_amdgcn_permlane16_swap(v[0], v[2], false, false); const u32 p0 = p[0], p1 = p[1];
  auto q = __builtin_amdgcn_permlane16_swap(v[1], v[3], false, false); const u32 q0 = q[0], q1 = q[1];
  return u32x4{p0, q0, p1, q1};
}
#define ZT 0
#define PSL 0
DEV u32 san(u32 v) { return min(v, SENT - 1u); }
DEV u32 mx4(u32x4 v) { return max(max(v[0], v[1]), max(v[2], v[3])); }
#define PIN(x) asm volatile("" : "+v"(x))
#define LANE() int l = tid0 & 63, w = w0, rot = rot0, rot2 = rot20; PIN(l); asm volatile("" : "+s"(w), "+s"(rot), "+s"(rot2)); const int c = l & 15, g = l >> 4, tid = w * 64 + l; (void)c; (void)g; (void)tid
#define SB() __builtin_amdgcn_sched_barrier(0)
#define DRAIN() asm volatile("s_waitcnt vmcnt(0)" ::: "memory")
DEV u32x4 poll4(rsrc_t r, u32 v, u32 s, int& cap) {
  u32x4 d; int n = 0;
  for (;;) { asm volatile("" ::: "memory"); d = ld4<16>(r, v, s); if (!__any(mx4(d) == SENT) || ++n >= cap) break; if (PSL) __builtin_amdgcn_s_sleep(PSL); }
  if (n >= cap) cap = 0;
  return d;
}
#define NSH 32
DEV void arrive(gu32* c) { DRAIN(); __syncthreads(); if (threadIdx.x == 0) __hip_atomic_fetch_add(c, 1u, __ATOMIC_RELAXED, __HIP_MEMORY_SCOPE_AGENT); }
DEV void await(gu32* c, int last, int& cap, int w, int l) {
  if (w == 0) {
    const int i = l & (NSH - 1); const u32 n = (last - i + NSH) / NSH; int k = 0;
    while (__any(__hip_atomic_load(c + i * 64, __ATOMIC_RELAXED, __HIP_MEMORY_SCOPE_AGENT) < n) && ++k < cap) __builtin_amdgcn_s_sleep(1);
    if (k >= cap) cap = 0;
  }
  __syncthreads();
}

typedef u32x4 __attribute__((aligned(2))) u32x4u;
DEV void pre_tile(int t, const u16* wr, const u16* wi, const u16* wg, const u16* wu, const u16* wd, char* ws) {
  const u16* src; long A; int B, C, P, Q; float sc = 1.f; u32x4* dst; bool nyq = false;
  if (t < 16384) {
    const int k = 8 * ((t >> 3) & 31), id = (t >> 8) * 8 + (t & 7), m2 = id >> 6, g = (id >> 4) & 3, c = id & 15;
    src = wr + (m2 + 8 * (c + 64 * g)) * 257 + k; A = wi - wr; B = 128 * 257; C = 256 * 257; P = 512; Q = 2048; sc = 0x1p-19f;
    dst = (u32x4*)(ws + O_WT) + (k * 8 + m2) * 64 + (id & 63); nyq = k == 0;
  } else if (t < 16384 + 22016) {
    const int q = t - 16384, r = (q >> 3) / NF, F = (q >> 3) - NF * r, lam = 2 * (r & 30) + ((q >> 1) & 3);
    src = ((r & 1) ? wu : wg) + 16 * F + 8 * (q & 1) + (16 * (lam & 15) + 4 * (lam >> 4)) * 1376; A = 1376; B = 2752; C = 256 * 1376; P = 8; Q = 32;
    dst = (u32x4*)(ws + O_WG) + (F * 32 + r) * 64 + (q & 7);
  } else {
    const int q = t - 38400, nt = (q >> 3) & 15, F = q >> 7;
    src = wd + (16 * min(F, NF - 1) + 8 * ((q >> 2) & 1)) * 512 + 32 * nt + 8 * (q & 3); A = 512; B = 1024; C = 2048; P = 8; Q = 32; sc = F < NF ? 1.f : 0.f;
    dst = (u32x4*)(ws + O_WD) + (nt * 88 + F) * 64 + (q & 7);
  }
  u32x4 v[8];
#pragma unroll
  for (int j = 0; j < 8; ++j) v[j] = *reinterpret_cast<const u32x4u*>(src + (j & 1) * A + ((j >> 1) & 1) * B + (j >> 2) * C);
  if (nyq) {
#pragma unroll
    for (int j = 0; j < 8; ++j) v[j][0] = (v[j][0] & 0xffff0000u) | src[(j & 1) * A + ((j >> 1) & 1) * B + (j >> 2) * C + 256];
  }
#pragma unroll
  for (int e = 0; e < 8; ++e) {
    const float s = (e == 0 && nyq) ? 0.5f * sc : sc;
    u32x4 o;
#pragma unroll
    for (int p = 0; p < 4; ++p) { const u32 a = v[2 * p][e >> 1], b = v[2 * p + 1][e >> 1]; o[p] = (e & 1) ? pk(bhi(a) * s, bhi(b) * s) : pk(blo(a) * s, blo(b) * s); }
    dst[(e & 3) * P + (e >> 2) * Q] = o;
  }
}

template <bool POLL> DEV void phase_a1(u32x4 xd, u32 xs, rsrc_t rw, u32 zs, char* scr, int s1, int w, int l, int& cap SARG) {
  const int c = l & 15, g = l >> 4;
  const u32 xo = (s1 + 256 * w) * 1024 + l * 16;
  u32x4 Bc, Bs, Am; u32x2 G; float tc[2][4], ts[2][4];
#pragma unroll
  for (int p = 0; p < 4; ++p) {
    const float a0 = (float)(((8 * g + 2 * p) * c) & 31) * (1.f / 32), a1 = (float)(((8 * g + 2 * p + 1) * c) & 31) * (1.f / 32);
    Bc[p] = pk(cosr(a0), cosr(a1)); Bs[p] = pk(-sinr(a0), -sinr(a1));
  }
#pragma unroll
  for (int t = 0; t < 2; ++t)
#pragma unroll
    for (int i = 0; i < 4; ++i) { const float a = (float)((c + 16 * t) * (4 * g + i)) * (1.f / 512); tc[t][i] = cosr(a); ts[t][i] = sinr(a); PIN(tc[t][i]); PIN(ts[t][i]); }
  {
    float cs[4], sn[4];
#pragma unroll
    for (int j = 0; j < 4; ++j) { const float a = (float)(((4 * g + j) * (c >> 1)) & 15) * (1.f / 16); cs[j] = cosr(a); sn[j] = sinr(a); }
    const u32 c0 = pk(cs[0], cs[1]), c1 = pk(cs[2], cs[3]), s0 = pk(sn[0], sn[1]), s1_ = pk(sn[2], sn[3]);
    Am = (c & 1) ? u32x4{s0 ^ SGN_BOTH, s1_ ^ SGN_BOTH, c0, c1} : u32x4{c0, c1, s0, s1_};
#pragma unroll
    for (int q = 0; q < 2; ++q) {
      const float a = (float)(((c >> 1) * (s1 + 256 * (2 * g + q))) & 2047) * (1.f / 2048), cc = cosr(a), ss = sinr(a);
      G[q] = (c & 1) ? pk(-ss, cc) : pk(cc, ss);
    }
  }
  PIN(Bc); PIN(Bs); PIN(Am); PIN(G); SB();
  if (POLL) { xd = poll4(rw, xo, xs, cap); STAMP(10); }
  char* tr = scr + 16384 + w * 1280;
  *reinterpret_cast<u32x4*>(tr + g * 288 + c * 16) = xd;
  float v[8], s = 0.f;
#pragma unroll
  for (int p = 0; p < 4; ++p) s += blo(xd[p]) + bhi(xd[p]);
  __builtin_amdgcn_fence(__ATOMIC_RELEASE, "wavefront"); __builtin_amdgcn_wave_barrier(); __builtin_amdgcn_fence(__ATOMIC_ACQUIRE, "wavefront");
#pragma unroll
  for (int j = 0; j < 8; ++j) v[j] = blo(*reinterpret_cast<const u16*>(tr + g * 288 + j * 32 + c * 2));
  const float mean = wave_sum(s) * (1.f / 512);
  float q = 0.f;
#pragma unroll
  for (int j = 0; j < 8; ++j) { v[j] -= mean; q += v[j] * v[j]; }
  const float rs = __builtin_amdgcn_rsqf(wave_sum(q) * (1.f / 512) + 1e-5f);
  const u32x4 a = {pk(v[0] * rs, v[1] * rs), pk(v[2] * rs, v[3] * rs), pk(v[4] * rs, v[5] * rs), pk(v[6] * rs, v[7] * rs)};
  const u32 ny = c == 0 ? 0xBF803F80u : 0u;
  u32* L = reinterpret_cast<u32*>(scr);
#pragma unroll
  for (int t = 0; t < 2; ++t) {
    const u32 f = t ? SGN_HI : 0u;
    const f32x4 dr = mma32(a, Bc ^ f, Z4), di = mma32(a, Bs ^ f, Z4);
    f32x4 re, im;
#pragma unroll
    for (int i = 0; i < 4; ++i) { re[i] = dr[i] * tc[t][i] + di[i] * ts[t][i]; im[i] = di[i] * tc[t][i] - dr[i] * ts[t][i]; }
    const u32x4 b = packc(re, im);
    f32x4 R = mma32(Am, b, Z4);
    if (t == 0) { const f32x4 n = mma32(u32x4{ny, ny, 0u, 0u}, b, Z4); if (l == 0) { R[0] = n[0]; R[1] = 0.f; } }
#pragma unroll
    for (int q2 = 0; q2 < 2; ++q2) L[((w >> 1) * 264 + c + 16 * t + 32 * (2 * g + q2)) * 2 + (w & 1)] = pk(R[2 * q2], R[2 * q2 + 1]);
  }
  asm volatile("s_waitcnt lgkmcnt(0)" ::: "memory"); __builtin_amdgcn_s_barrier(); asm volatile("" ::: "memory");
  const f32x4 o0 = mma16(G, reinterpret_cast<const u32x2*>(L)[g * 264 + c + 32 * w], Z4), o1 = mma16(G, reinterpret_cast<const u32x2*>(L)[g * 264 + c + 32 * w + 16], Z4);
  st4<(POLL || ZT) ? 16 : 0>(swp(u32x4{san(pk(o0[0], o0[1])), san(pk(o0[2], o0[3])), san(pk(o1[0], o1[1])), san(pk(o1[2], o1[3]))}), rw,
                     ((c + 32 * w + 16 * (g & 1)) * 256 + s1) * 32 + (g & 2) * 8, zs);
}

__global__ __launch_bounds__(768) void k_pre(const u16* __restrict__ x, const u16* __restrict__ wr, const u16* __restrict__ wi, const u16* __restrict__ wg,
                                                const u16* __restrict__ wu, const u16* __restrict__ wd, char* __restrict__ ws DARG) {
  __shared__ __attribute__((aligned(16))) char scr[16384 + 8 * 1280 + 256];
  u32 stv = 0; (void)stv;
  const int b = blockIdx.x, tid = threadIdx.x, w = __builtin_amdgcn_readfirstlane(tid >> 6);
  STAMP(0);
  if (w >= 8) {
    if (w == 11) {
#pragma unroll
      for (int i = 0; i < 8; ++i) st4<16>(u32x4{SENT, SENT, SENT, SENT}, mkrs(ws, WS_END), (tid & 63) * 16, O_Y1 + (b * 8 + i) * 1024);
    }
    if (w < 11 || b < 8) pre_tile(((w - 8) * 256 + b) * 64 + (tid & 63), wr, wi, wg, wu, wd, ws);
    else if (b < 24) {
      const char *c0, *c1;
      asm volatile("s_getpc_b64 vcc\n\ts_add_u32 vcc_lo, vcc_lo, k_main@rel32@lo+4\n\ts_addc_u32 vcc_hi, vcc_hi, k_main@rel32@hi+12\n\ts_mov_b64 %0, vcc" : "=s"(c0) :: "vcc", "scc");
      asm volatile("s_getpc_b64 vcc\n\ts_add_u32 vcc_lo, vcc_lo, k_end@rel32@lo+4\n\ts_addc_u32 vcc_hi, vcc_hi, k_end@rel32@hi+12\n\ts_mov_b64 %0, vcc" : "=s"(c1) :: "vcc", "scc");
      const long n = c1 - c0;
      for (long o = (tid & 63) * 128; n < 65536 && o < n; o += 8192) { u32 d; asm volatile("global_load_dword %0, %1, off\n\ts_waitcnt vmcnt(0)" : "=v"(d) : "v"(c0 + o) : "memory"); }
    }
    return;
  }
  const rsrc_t rw = mkrs(ws, WS_END), rx = mkrs(x, 2 * MB);
  if (tid < 4) *reinterpret_cast<u32*>(ws + O_CNT + (b * 4 + tid) * 256) = 0u;
  int cap = 0;
  const int s1 = (b & 7) * 32 + (b >> 3);
  const u32x4 xd = ld4<0>(rx, (s1 + 256 * w) * 1024 + (tid & 63) * 16, 0);
  phase_a1<false>(xd, 0, rw, O_Z1, scr, s1, w, tid & 63, cap SPASS);
  STAMP(1); VW(); STAMP(4); FLUSH(19);
}

DEV void glds16(const void* gsrc, u32 lds_dst) {
  u32 keep;
  asm volatile("s_mov_b32 %0, m0\n\ts_mov_b32 m0, %2\n\ts_nop 0\n\tglobal_load_lds_dwordx4 %1, off\n\ts_mov_b32 m0, %0" : "=&s"(keep) : "v"(gsrc), "s"(lds_dst) : "memory");
}
#define XW 10
#define XA 16
#define PRIO 0
#define HW 7
#define WD0 8

extern "C" __global__ __launch_bounds__(512, 2) void k_main(const u16* __restrict__ x, char* __restrict__ ws, u16* __restrict__ out DARG) {
  extern __shared__ __attribute__((aligned(16))) u32x4 SM[];
  char* scr = reinterpret_cast<char*>(SM) + 98304;
  u32 stv = 0; (void)stv;
  const int b = blockIdx.x, tid0 = threadIdx.x, w0 = __builtin_amdgcn_readfirstlane(tid0 >> 6);
  const int grp = b & 7, r_ = b >> 3, s1 = grp * 32 + r_;
  const rsrc_t rw = mkrs(ws, WS_END), rx0 = mkrs(x, 2 * MB), ro = mkrs(out, 2 * MB);
  int cap = SPIN_CAP;
  STAMP(0);
  u32x4 wt, zd;
  asm volatile("global_load_dwordx4 %0, %2, off\n\tglobal_load_dwordx4 %1, %3, off" : "=&v"(wt), "=&v"(zd)
               : "v"(ws + O_WT + s1 * 8192 + tid0 * 16), "v"(ws + O_Z1 + s1 * 8192 + tid0 * 16) : "memory");
  st4<16>(u32x4{SENT, SENT, SENT, SENT}, rw, tid0 * 16, O_Z2 + s1 * 8192);
  const int NG = r_ & 7, TG = grp * 4 + (r_ >> 3), F0 = r_ * 3, rot0 = (r_ * 10) & 30, rot20 = r_ < 29 ? (NG * 11) >> 3 : 0;
#define FSTEP(s) (11 * w + (s) + rot2 - ((s) + rot2 >= 11 ? 11 : 0))
#define LDWD1(s, h) asm volatile("s_cmp_lg_u32 %4, 0\n\ts_cbranch_scc1 1f\n\tbuffer_load_dwordx4 %0, %1, %2, %3 offen\n1:" : "+v"(wd[s][h]) \
    : "v"(wdl), "s"(rw), "s"(O_WD + ((2 * NG + (h)) * 88 + FSTEP(s)) * 1024), "s"(L) : "scc")
#define LDWDALL(h) { u32 o_ = O_WD + ((2 * NG + (h)) * 88 + 11 * w) * 1024; asm volatile("s_cmp_lg_u32 %14, 0\n\ts_cbranch_scc1 1f\n\t" \
    "buffer_load_dwordx4 %0, %12, %13, %11 offen\n\ts_add_u32 %11, %11, 1024\n\tbuffer_load_dwordx4 %1, %12, %13, %11 offen\n\ts_add_u32 %11, %11, 1024\n\tbuffer_load_dwordx4 %2, %12, %13, %11 offen\n\ts_add_u32 %11, %11, 1024\n\tbuffer_load_dwordx4 %3, %12, %13, %11 offen\n\ts_add_u32 %11, %11, 1024\n\tbuffer_load_dwordx4 %4, %12, %13, %11 offen\n\ts_add_u32 %11, %11, 1024\n\tbuffer_load_dwordx4 %5, %12, %13, %11 offen\n\ts_add_u32 %11, %11, 1024\n\tbuffer_load_dwordx4 %6, %12, %13, %11 offen\n\ts_add_u32 %11, %11, 1024\n\tbuffer_load_dwordx4 %7, %12, %13, %11 offen\n\ts_add_u32 %11, %11, 1024\n\tbuffer_load_dwordx4 %8, %12, %13, %11 offen\n\ts_add_u32 %11, %11, 1024\n\tbuffer_load_dwordx4 %9, %12, %13, %11 offen\n\ts_add_u32 %11, %11, 1024\n\tbuffer_load_dwordx4 %10, %12, %13, %11 offen\n\ts_add_u32 %11, %11, 1024\n\ts_waitcnt vmcnt(0)\n1:" \
    : "+v"(wd[0][h]), "+v"(wd[1][h]), "+v"(wd[2][h]), "+v"(wd[3][h]), "+v"(wd[4][h]), "+v"(wd[5][h]), "+v"(wd[6][h]), "+v"(wd[7][h]), "+v"(wd[8][h]), "+v"(wd[9][h]), "+v"(wd[10][h]), "+s"(o_) : "v"(wdl), "s"(rw), "s"(L) : "scc"); }
  const u32 lb = (u32)(size_t)(__attribute__((address_space(3))) u32x4*)SM;
#define LDGU(i) { const char* src = ws + O_WG + (min(F0 + (i), NF - 1) * 32 + w * 4) * 1024 + (l & 7) * 128 + (l >> 3) * 16; \
    _Pragma("unroll") for (int j = 0; j < 4; ++j) glds16(src + j * 1024, lb + (((4 * w + j - rot) & 31) * 3 + (i)) * 1024); }

  u32x4 wd[11][2];
  for (int L = 0; L < 2; ++L) {
    {
      LANE();

      float cs[4], sn[4], tc[4], ts[4];
#pragma unroll
      for (int j = 0; j < 4; ++j) {
        const int p = (4 * g + j) * c;
        const float a = (float)(p & 15) * (1.f / 16), bb = (float)p * (1.f / 256);
        cs[j] = cosr(a); sn[j] = sinr(a); tc[j] = cosr(bb); ts[j] = sinr(bb); PIN(tc[j]); PIN(ts[j]);
      }
      u32x4 B1r = {pk(cs[0], sn[0]), pk(cs[1], sn[1]), pk(cs[2], sn[2]), pk(cs[3], sn[3])};
      u32x4 B1i = {pk(-sn[0], cs[0]), pk(-sn[1], cs[1]), pk(-sn[2], cs[2]), pk(-sn[3], cs[3])};
      u32x4 Fr = {pk(cs[0], cs[1]), pk(cs[2], cs[3]), pk(sn[0], sn[1]), pk(sn[2], sn[3])};
      PIN(B1r); PIN(B1i); PIN(Fr); SB();
      if (L == 1) zd = poll4(rw, tid * 16, O_Z2 + s1 * 8192, cap);
      asm volatile("s_waitcnt vmcnt(1)" : "+v"(wt), "+v"(zd) :: "memory");
      u32* P = reinterpret_cast<u32*>(scr + 8448);
#pragma unroll
      for (int i = 0; i < 4; ++i) { const int sa = tid >> 1; P[(4 * (tid & 1) + i) * 312 + sa + 16 * (sa >> 6)] = zd[i]; }
      if (L == 0) LDGU(0)
      asm volatile("s_waitcnt lgkmcnt(0)" ::: "memory"); __builtin_amdgcn_s_barrier(); asm volatile("" ::: "memory");
      STAMP(1 + 12 * L);
      u32x4 a;
#pragma unroll
      for (int j = 0; j < 4; ++j) a[j] = P[w * 312 + 80 * g + 16 * j + c];
      const u32x4 Fi = {Fr[2] ^ SGN_BOTH, Fr[3] ^ SGN_BOTH, Fr[0], Fr[1]}, Ir = {Fr[0], Fr[1], Fi[0], Fi[1]}, Ii = {Fr[2], Fr[3], Fr[0], Fr[1]};
      f32x4 dr = mma32(a, B1r, Z4), di = mma32(a, B1i, Z4), re, im;
#pragma unroll
      for (int i = 0; i < 4; ++i) { re[i] = dr[i] * tc[i] + di[i] * ts[i]; im[i] = di[i] * tc[i] - dr[i] * ts[i]; }
      u32x4 bb = packc(re, im);
      dr = mma32(Fr, bb, Z4); di = mma32(Fi, bb, Z4);
#pragma unroll
      for (int i = 0; i < 4; ++i) { const float wr = blo(wt[i]), wi = bhi(wt[i]); re[i] = dr[i] * wr - di[i] * wi; im[i] = di[i] * wr + dr[i] * wi; }
      bb = packc(re, im);
      dr = mma32(bb, Ir, Z4); di = mma32(bb, Ii, Z4);
#pragma unroll
      for (int i = 0; i < 4; ++i) { re[i] = dr[i] * tc[i] - di[i] * ts[i]; im[i] = di[i] * tc[i] + dr[i] * ts[i]; }
      bb = packc(re, im);
      dr = mma32(Ir, bb, Z4); di = mma32(Ii, bb, Z4);
      u32* Q = reinterpret_cast<u32*>(scr + 20480);
#pragma unroll
      for (int i = 0; i < 4; ++i) Q[(16 * (4 * g + i) + c) * 8 + (w ^ (((c >> 2) & 1) * 4))] = san(pk(dr[i], di[i]));
      asm volatile("s_waitcnt vmcnt(4) lgkmcnt(0)" ::: "memory");   __builtin_amdgcn_s_barrier(); asm volatile("" ::: "memory");
      st4<16>(reinterpret_cast<const u32x4*>(Q)[tid ^ ((tid >> 3) & 1)], rw, ((tid >> 1) * 256 + s1) * 32 + (tid & 1) * 16, L ? O_Y2 : O_Y1);
      STAMP(2 + 12 * L);
    }
    {
      LANE();
      const u32 xo = (s1 + 256 * w) * 1024 + 32 * c + 8 * (g & 2) + 512 * (g & 1);
      u32x4 xr;
      if (L == 0) xr = ld4<0>(rx0, xo, 0); else xr = ld4<16>(rw, xo, O_X2);
      u32x2 G, Br, Bi; u32x4 B2; float tc[2][4], ts[2][4];
#pragma unroll
      for (int q = 0; q < 2; ++q) {
        const float a = (float)(((2 * g + q) * (s1 + 256 * (c >> 1))) & 2047) * (1.f / 2048), cc = cosr(a), ss = sinr(a);
        G[q] = (c & 1) ? pk(ss, cc) : pk(cc, -ss);
        const float bb = (float)(((2 * g + q) * c) & 15) * (1.f / 16), cb = cosr(bb), sb = sinr(bb);
        Br[q] = pk(cb, -sb); Bi[q] = pk(sb, cb);
      }
#pragma unroll
      for (int t = 0; t < 2; ++t)
#pragma unroll
        for (int i = 0; i < 4; ++i) { const float a = (float)((16 * t + 4 * g + i) * c) * (1.f / 512); tc[t][i] = cosr(a); ts[t][i] = sinr(a); PIN(tc[t][i]); PIN(ts[t][i]); }
      {
        float cs[4], sn[4];
#pragma unroll
        for (int j = 0; j < 4; ++j) { const float a = (float)(((4 * g + j) * c) & 31) * (1.f / 32); cs[j] = cosr(a); sn[j] = -sinr(a); }
        B2 = u32x4{pk(cs[0], cs[1]), pk(cs[2], cs[3]), pk(sn[0], sn[1]), pk(sn[2], sn[3])};
      }
      PIN(G); PIN(Br); PIN(Bi); PIN(B2); SB();
      const u32x4 y = swp(poll4(rw, (s1 * 256 + c + 32 * w + 16 * (g & 1)) * 32 + (g & 2) * 8, L ? O_Y2 : O_Y1, cap));
      if (L == 0) {
        st4<16>(u32x4{SENT, SENT, SENT, SENT}, rw, tid * 16, O_Y2 + s1 * 8192);
        st4<16>(u32x4{SENT, SENT, SENT, SENT}, rw, l * 16, O_X2 + (s1 + 256 * w) * 1024);
        LDGU(1) LDGU(2)
      }
      STAMP(3 + 12 * L);
      u32x2* Lp = reinterpret_cast<u32x2*>(scr);
#pragma unroll
      for (int u = 0; u < 2; ++u) { const f32x4 o = mma16(G, u32x2{y[2 * u], y[2 * u + 1]}, Z4); Lp[g * 264 + c + 16 * (2 * w + u)] = u32x2{pk(o[0], o[1]), pk(o[2], o[3])}; }
      asm volatile("s_waitcnt lgkmcnt(0)" ::: "memory"); __builtin_amdgcn_s_barrier(); asm volatile("" ::: "memory");
      u32 r[2][2];
#pragma unroll
      for (int t = 0; t < 2; ++t)
#pragma unroll
        for (int q = 0; q < 2; ++q) r[t][q] = reinterpret_cast<const u32*>(scr)[((w >> 1) * 264 + c + 16 * t + 32 * (2 * g + q)) * 2 + (w & 1)];
      const float nyq = blo(__builtin_amdgcn_readfirstlane(r[0][0]));
      if (l == 0) r[0][0] = 0u;
      f32x4 acc[2];
      xr = swp(xr);
#pragma unroll
      for (int t = 0; t < 2; ++t) acc[t] = f32x4{blo(xr[2 * t]), bhi(xr[2 * t]), blo(xr[2 * t + 1]), bhi(xr[2 * t + 1])};
#pragma unroll
      for (int t = 0; t < 2; ++t) {
        const u32x2 a = {r[t][0], r[t][1]};
        f32x4 dr = mma16(a, Br, Z4), di = mma16(a, Bi, Z4), re, im;
        if (t == 0) dr[0] += g == 0 ? ((c & 1) ? -nyq : nyq) : 0.f;
#pragma unroll
        for (int i = 0; i < 4; ++i) { re[i] = dr[i] * tc[t][i] - di[i] * ts[t][i]; im[i] = di[i] * tc[t][i] + dr[i] * ts[t][i]; }
        const u32x4 a2 = packc(re, im);
        const u32 f = (t && (c & 1)) ? SGN_BOTH : 0u;
        acc[0] = mma32(a2, B2 ^ f, acc[0]); acc[1] = mma32(a2, B2 ^ (f ^ SGN_HI), acc[1]);
      }
      const u32x4 xq = packc(acc[0], acc[1]);
      st4<16>(swp(xq), rw, xo, O_X1 + L * 2 * MB);
      float v[8], s = 0.f;
#pragma unroll
      for (int p = 0; p < 4; ++p) { v[2 * p] = blo(xq[p]); v[2 * p + 1] = bhi(xq[p]); s += v[2 * p] + v[2 * p + 1]; }
      const float mean = wave_sum(s) * (1.f / 512);
      float q = 0.f;
#pragma unroll
      for (int j = 0; j < 8; ++j) { v[j] -= mean; q += v[j] * v[j]; }
      const float rs = __builtin_amdgcn_rsqf(wave_sum(q) * (1.f / 512) + 1e-5f);
      u32x4* Xt = reinterpret_cast<u32x4*>(scr + 8448);
      Xt[l * 8 + (w ^ (l & 7))] = u32x4{pk(v[0] * rs, v[1] * rs), pk(v[2] * rs, v[3] * rs), pk(v[4] * rs, v[5] * rs), pk(v[6] * rs, v[7] * rs)};
      asm volatile("s_waitcnt lgkmcnt(0)" ::: "memory"); __builtin_amdgcn_s_barrier(); asm volatile("" ::: "memory");
      st4<16>(Xt[tid ^ ((tid >> 3) & 7)], rw, ((s1 >> 2) * 2048 + (tid >> 3) * 32 + (s1 & 3) * 8 + (tid & 7)) * 16, O_XN + L * 2 * MB);
      STAMP(4 + 12 * L);
    }
    gu32* cnt = (gu32*)(ws + O_CNT + (L * 16 + grp) * 8192);
    arrive(cnt + (r_ & (NSH - 1)) * 64); STAMP(24 + 4 * L);
    LANE();
    const u32 wdl = (((l >> 3) & 1) * 4 + (l & 3)) * 128 + ((l >> 5) * 4 + ((l >> 2) & 1) * 2 + ((l >> 4) & 1)) * 16;
    if (r_ < 29) {
      await(cnt, 31, cap, w, l);
      STAMP(5 + 12 * L);
      const u32 xo = (grp * 8 + w) * 32768 + g * 512 + c * 16, xs = O_XN + L * 2 * MB;
#define LDX(n) xf[n] = ld4<16>(rw, xo + ((n) & 1) * 256, xs + ((((n) & 30) + rot) & 31) * 1024)
      u32x4 xf[32], wf[2][3];
#pragma unroll
      for (int i = 0; i < XW; ++i) LDX(i);
#pragma unroll
      for (int i = 0; i < 3; ++i) wf[0][i] = SM[i * 64 + l];
      f32x4 acc[2][3][2] = {};
      if (w < 4) __builtin_amdgcn_s_setprio(PRIO);
      SB();
#define B1LOOP(W) _Pragma("unroll") for (int ks = 0; ks < 32; ++ks) { \
        if (ks + XW < 32) LDX(ks + XW); \
        if (ks >= WD0 && ks < WD0 + 22) LDWD1((ks - WD0) >> 1, (ks - WD0) & 1); \
        if (ks + 1 < 32) { _Pragma("unroll") for (int i = 0; i < 3; ++i) wf[(ks + 1) & 1][i] = SM[((ks + 1) * 3 + i) * 64 + l]; } \
        SB(); \
        _Pragma("unroll") for (int i = 0; i < 6; ++i) acc[ks & 1][i >> 1][i & 1] = mma32(wf[ks & 1][i >> 1], xf[(ks & 30) + (i & 1)], acc[ks & 1][i >> 1][i & 1]); \
        SB(); }
      B1LOOP(1)
#pragma unroll
      for (int i = 0; i < 3; ++i) {
        float h[8];
#pragma unroll
        for (int r = 0; r < 8; ++r) h[r] = fmaxf(acc[0][i][r >> 2][r & 3], 0.f) * acc[1][i][r >> 2][r & 3];
        if (F0 + i < NF) st4<16>(swp(u32x4{pk(h[0], h[1]), pk(h[2], h[3]), pk(h[4], h[5]), pk(h[6], h[7])}), rw, ((grp * 8 + w) * NF + F0 + i) * 1024 + l * 16, O_H + L * 6 * MB);
      }
      __builtin_amdgcn_s_setprio(0);
      STAMP(6 + 12 * L);
      arrive(cnt + 8 * 2048 + (r_ & (NSH - 1)) * 64); STAMP(25 + 4 * L);
    }
    else { LDWDALL(0) LDWDALL(1) }
    {
      await(cnt + 8 * 2048, 28, cap, w, l);
      STAMP(7 + 12 * L);
      const u32 hs = O_H + L * 6 * MB + 2 * TG * NF * 1024;
      const u32 oo = (8 * TG + w + 256 * (l >> 3)) * 1024 + NG * 128 + (l & 7) * 16;
      u32x4 hf[11][2];
#define LDH(s) { const u32 o_ = hs + min(FSTEP(s), NF - 1) * 1024; hf[s][0] = ld4<16>(rw, l * 16, o_); hf[s][1] = ld4<16>(rw, l * 16, o_ + NF * 1024); }
#pragma unroll
      for (int s = 0; s < HW; ++s) LDH(s)
      f32x16 acc[4] = {};
      u32x4 res;
      SB();
#pragma unroll
      for (int s = 0; s < 11; ++s) {
        if (s + HW < 11) LDH(s + HW)
        if (s + HW == 11) res = ld4<16>(rw, oo, O_X1 + L * 2 * MB);
        SB();
#pragma unroll
        for (int q = 0; q < 4; ++q) acc[q] = mmb(wd[s][q & 1], hf[s][q >> 1], acc[q]);
        SB();
      }
      STAMP(8 + 12 * L);
      u32x4* Rd = reinterpret_cast<u32x4*>(scr);
#pragma unroll
      for (int d = 0; d < 8; ++d) {
        const f32x16 A = acc[d >> 1]; const int o = 8 * (d & 1);
        Rd[w * 512 + (32 * (d >> 2) + (l & 31)) * 8 + ((4 * ((d >> 1) & 1) + 2 * (l >> 5) + (d & 1)) ^ (l & 7))] =
            u32x4{pk(A[o], A[o + 1]), pk(A[o + 2], A[o + 3]), pk(A[o + 4], A[o + 5]), pk(A[o + 6], A[o + 7])};
      }
      asm volatile("s_waitcnt lgkmcnt(0)" ::: "memory"); __builtin_amdgcn_s_barrier(); asm volatile("" ::: "memory");
      STAMP(26 + 4 * L);
      float own[8] = {};
#pragma unroll
      for (int j = 0; j < 8; ++j) {
        const u32x4 p = Rd[j * 512 + w * 64 + (l ^ (l >> 3))];
#pragma unroll
        for (int i = 0; i < 4; ++i) { own[2 * i] += blo(p[i]); own[2 * i + 1] += bhi(p[i]); }
      }
      u32x4 o;
#pragma unroll
      for (int i = 0; i < 4; ++i) o[i] = pk(own[2 * i] + blo(res[i]), own[2 * i + 1] + bhi(res[i]));
      if (L == 1) { st4<0>(o, ro, oo, 0); STAMP(21); VW(); STAMP(22); FLUSH(r_ < 29 ? 0x77FFEFFFu : 0x557FEFFFu & ~0x60060u); break; }
      st4<16>(u32x4{san(o[0]), san(o[1]), san(o[2]), san(o[3])}, rw, oo, O_X2);
      asm volatile("s_waitcnt lgkmcnt(0)" ::: "memory"); __builtin_amdgcn_s_barrier(); asm volatile("" ::: "memory");
      STAMP(9);
    }
    { LANE(); phase_a1<true>(zd, O_X2, rw, O_Z2, scr, s1, w, l, cap SPASS); STAMP(11); }
  }
}

extern "C" __global__ void k_end() {}

extern "C" void kernel_launch(void* const* d_in, const int* in_sizes, int n_in, void* d_out, int out_size,
                              void* d_ws, size_t ws_size, hipStream_t stream) {
  (void)hipFuncSetAttribute((const void*)k_main, hipFuncAttributeMaxDynamicSharedMemorySize, 163840);
  k_pre<<<256, 768, 0, stream>>>((const u16*)d_in[0], (const u16*)d_in[1], (const u16*)d_in[2], (const u16*)d_in[4], (const u16*)d_in[5],
                                       (const u16*)d_in[6], (char*)d_ws DPASS(0));
  k_main<<<256, 512, 163840, stream>>>((const u16*)d_in[0], (char*)d_ws, (u16*)d_out DPASS(1));
}
```

```cpp
#include <hip/hip_runtime.h>
#include <hip/hip_bf16.h>

typedef unsigned short u16;
typedef unsigned int u32;
typedef float f32x2 __attribute__((ext_vector_type(2)));
typedef float f32x4 __attribute__((ext_vector_type(4)));
typedef float f32x16 __attribute__((ext_vector_type(16)));
typedef u32 u32x2 __attribute__((ext_vector_type(2)));
typedef u32 u32x4 __attribute__((ext_vector_type(4)));
typedef __bf16 bf2 __attribute__((ext_vector_type(2)));
typedef __bf16 bf8 __attribute__((ext_vector_type(8)));
typedef short s16x4 __attribute__((ext_vector_type(4)));

#define DEV __device__ __forceinline__
#define DARG
#define DPASS(n)
#define STAMP(i)
#define FLUSH(m)
#define SARG
#define SPASS
#define VW()
constexpr int NF = 86;
constexpr u32 SGN_HI = 0x80000000u, SGN_LO = 0x00008000u, SGN_BOTH = 0x80008000u;

DEV u32 pk(float a, float b) { f32x2 v = {a, b}; return __builtin_bit_cast(u32, __builtin_convertvector(v, bf2)); }
DEV float blo(u32 v) { return __builtin_bit_cast(float, v << 16); }
DEV float bhi(u32 v) { return __builtin_bit_cast(float, v & 0xffff0000u); }
DEV float cosr(float r) { return __builtin_amdgcn_cosf(r); }
DEV float sinr(float r) { return __builtin_amdgcn_sinf(r); }
DEV f32x4 mma32(u32x4 a, u32x4 b, f32x4 c) { return __builtin_amdgcn_mfma_f32_16x16x32_bf16(__builtin_bit_cast(bf8, a), __builtin_bit_cast(bf8, b), c, 0, 0, 0); }
DEV f32x4 mma16(u32x2 a, u32x2 b, f32x4 c) { return __builtin_amdgcn_mfma_f32_16x16x16bf16_1k(__builtin_bit_cast(s16x4, a), __builtin_bit_cast(s16x4, b), c, 0, 0, 0); }
DEV f32x16 mmb(u32x4 a, u32x4 b, f32x16 c) { return __builtin_amdgcn_mfma_f32_32x32x16_bf16(__builtin_bit_cast(bf8, a), __builtin_bit_cast(bf8, b), c, 0, 0, 0); }
template <int CTRL> DEV float dpp(float v) { return __builtin_bit_cast(float, __builtin_amdgcn_update_dpp(0, __builtin_bit_cast(int, v), CTRL, 0xf, 0xf, true)); }
DEV float wave_sum(float v) {
  v += dpp<0xB1>(v); v += dpp<0x4E>(v); v += dpp<0x141>(v); v += dpp<0x140>(v);
  u32 u = __builtin_bit_cast(u32, v);
  auto r = __builtin_amdgcn_permlane16_swap(u, u, false, false);
  const u32 r0 = r[0], r1 = r[1];
  v = __builtin_bit_cast(float, r0) + __builtin_bit_cast(float, r1);
  u = __builtin_bit_cast(u32, v);
  auto q = __builtin_amdgcn_permlane32_swap(u, u, false, false);
  const u32 q0 = q[0], q1 = q[1];
  return __builtin_bit_cast(float, q0) + __builtin_bit_cast(float, q1);
}
DEV u32x4 packc(const f32x4& re, const f32x4& im) { return u32x4{pk(re[0], re[1]), pk(re[2], re[3]), pk(im[0], im[1]), pk(im[2], im[3])}; }
const f32x4 __device__ Z4 = {0.f, 0.f, 0.f, 0.f};

constexpr u32 MB = 1u << 20;
constexpr u32 O_Z1 = 0, O_Y1 = 2 * MB, O_Z2 = 4 * MB, O_Y2 = 6 * MB, O_X2 = 8 * MB, O_WT = 10 * MB, O_X1 = 12 * MB  ,
              O_XN = 16 * MB  , O_WG = 20 * MB, O_WD = 23 * MB, O_CNT = 25 * MB, O_H = 26 * MB  , WS_END = 38 * MB;
constexpr u32 SENT = 0xFFFFFFFFu;
constexpr int SPIN_CAP = 1 << 18;
typedef __amdgpu_buffer_rsrc_t rsrc_t;
typedef __attribute__((address_space(1))) u32 gu32;
DEV rsrc_t mkrs(const void* p, u32 n) { return __builtin_amdgcn_make_buffer_rsrc(const_cast<void*>(p), 0, n, 0x00020000); }
template <int AUX> DEV u32x4 ld4(rsrc_t r, u32 v, u32 s) { return __builtin_amdgcn_raw_buffer_load_b128(r, v, s, AUX); }
template <int AUX> DEV u32x2 ld2(rsrc_t r, u32 v, u32 s) { return __builtin_amdgcn_raw_buffer_load_b64(r, v, s, AUX); }
template <int AUX> DEV void st4(u32x4 d, rsrc_t r, u32 v, u32 s) { __builtin_amdgcn_raw_buffer_store_b128(d, r, v, s, AUX); }
template <int AUX> DEV void st2(u32x2 d, rsrc_t r, u32 v, u32 s) { __builtin_amdgcn_raw_buffer_store_b64(d, r, v, s, AUX); }
DEV u32x4 swp(u32x4 v) {
  auto p = __builtin_amdgcn_permlane16_swap(v[0], v[2], false, false); const u32 p0 = p[0], p1 = p[1];
  auto q = __builtin_amdgcn_permlane16_swap(v[1], v[3], false, false); const u32 q0 = q[0], q1 = q[1];
  return u32x4{p0, q0, p1, q1};
}
#define ZT 0
#define PSL 0
DEV u32 san(u32 v) { return min(v, SENT - 1u); }
DEV u32 mx4(u32x4 v) { return max(max(v[0], v[1]), max(v[2], v[3])); }
#define PIN(x) asm volatile("" : "+v"(x))
#define LANE() int l = tid0 & 63, w = w0, rot = rot0, rot2 = rot20; PIN(l); asm volatile("" : "+s"(w), "+s"(rot), "+s"(rot2)); const int c = l & 15, g = l >> 4, tid = w * 64 + l; (void)c; (void)g; (void)tid
#define SB() __builtin_amdgcn_sched_barrier(0)
#define DRAIN() asm volatile("s_waitcnt vmcnt(0)" ::: "memory")
DEV u32x4 poll4(rsrc_t r, u32 v, u32 s, int& cap) {
  u32x4 d; int n = 0;
  for (;;) { asm volatile("" ::: "memory"); d = ld4<16>(r, v, s); if (!__any(mx4(d) == SENT) || ++n >= cap) break; if (PSL) __builtin_amdgcn_s_sleep(PSL); }
  if (n >= cap) cap = 0;
  return d;
}
#define NSH 32
DEV void arrive(gu32* c) { DRAIN(); __syncthreads(); if (threadIdx.x == 0) __hip_atomic_fetch_add(c, 1u, __ATOMIC_RELAXED, __HIP_MEMORY_SCOPE_AGENT); }
DEV void await(gu32* c, int last, int& cap, int w, int l) {
  if (w == 0) {
    const int i = l & (NSH - 1); const u32 n = (last - i + NSH) / NSH; int k = 0;
    while (__any(__hip_atomic_load(c + i * 64, __ATOMIC_RELAXED, __HIP_MEMORY_SCOPE_AGENT) < n) && ++k < cap) __builtin_amdgcn_s_sleep(1);
    if (k >= cap) cap = 0;
  }
  __syncthreads();
}

typedef u32x4 __attribute__((aligned(2))) u32x4u;
DEV void pre_tile(int t, const u16* wr, const u16* wi, const u16* wg, const u16* wu, const u16* wd, char* ws) {
  const u16* src; long A; int B, C, P, Q; float sc = 1.f; u32x4* dst; bool nyq = false;
  if (t < 16384) {
    const int k = 8 * ((t >> 3) & 31), id = (t >> 8) * 8 + (t & 7), m2 = id >> 6, g = (id >> 4) & 3, c = id & 15;
    src = wr + (m2 + 8 * (c + 64 * g)) * 257 + k; A = wi - wr; B = 128 * 257; C = 256 * 257; P = 512; Q = 2048; sc = 0x1p-19f;
    dst = (u32x4*)(ws + O_WT) + (k * 8 + m2) * 64 + (id & 63); nyq = k == 0;
  } else if (t < 16384 + 22016) {
    const int q = t - 16384, r = (q >> 3) / NF, F = (q >> 3) - NF * r, lam = 2 * (r & 30) + ((q >> 1) & 3);
    src = ((r & 1) ? wu : wg) + 16 * F + 8 * (q & 1) + (16 * (lam & 15) + 4 * (lam >> 4)) * 1376; A = 1376; B = 2752; C = 256 * 1376; P = 1; Q = 4;
    dst = (u32x4*)(ws + O_WG) + (F * 32 + r) * 64 + (q & 7) * 8;
  } else {
    const int q = t - 38400, nt = (q >> 3) & 15, F = q >> 7;
    src = wd + (16 * min(F, NF - 1) + 8 * ((q >> 2) & 1)) * 512 + 32 * nt + 8 * (q & 3); A = 512; B = 1024; C = 2048; P = 1; Q = 8; sc = F < NF ? 1.f : 0.f;
    dst = (u32x4*)(ws + O_WD) + (nt * 88 + F) * 64 + 32 * ((q >> 2) & 1) + 16 * (q & 1) + 4 * ((q >> 1) & 1);
  }
  u32x4 v[8];
#pragma unroll
  for (int j = 0; j < 8; ++j) v[j] = *reinterpret_cast<const u32x4u*>(src + (j & 1) * A + ((j >> 1) & 1) * B + (j >> 2) * C);
  if (nyq) {
#pragma unroll
    for (int j = 0; j < 8; ++j) v[j][0] = (v[j][0] & 0xffff0000u) | src[(j & 1) * A + ((j >> 1) & 1) * B + (j >> 2) * C + 256];
  }
#pragma unroll
  for (int e = 0; e < 8; ++e) {
    const float s = (e == 0 && nyq) ? 0.5f * sc : sc;
    u32x4 o;
#pragma unroll
    for (int p = 0; p < 4; ++p) { const u32 a = v[2 * p][e >> 1], b = v[2 * p + 1][e >> 1]; o[p] = (e & 1) ? pk(bhi(a) * s, bhi(b) * s) : pk(blo(a) * s, blo(b) * s); }
    dst[(e & 3) * P + (e >> 2) * Q] = o;
  }
}

template <bool POLL> DEV void phase_a1(u32x4 xd, u32 xs, rsrc_t rw, u32 zs, char* scr, int s1, int w, int l, int& cap SARG) {
  const int c = l & 15, g = l >> 4;
  const u32 xo = (s1 + 256 * w) * 1024 + l * 16;
  u32x4 Bc, Bs, Am; u32x2 G; float tc[2][4], ts[2][4];
#pragma unroll
  for (int p = 0; p < 4; ++p) {
    const float a0 = (float)(((8 * g + 2 * p) * c) & 31) * (1.f / 32), a1 = (float)(((8 * g + 2 * p + 1) * c) & 31) * (1.f / 32);
    Bc[p] = pk(cosr(a0), cosr(a1)); Bs[p] = pk(-sinr(a0), -sinr(a1));
  }
#pragma unroll
  for (int t = 0; t < 2; ++t)
#pragma unroll
    for (int i = 0; i < 4; ++i) { const float a = (float)((c + 16 * t) * (4 * g + i)) * (1.f / 512); tc[t][i] = cosr(a); ts[t][i] = sinr(a); PIN(tc[t][i]); PIN(ts[t][i]); }
  {
    float cs[4], sn[4];
#pragma unroll
    for (int j = 0; j < 4; ++j) { const float a = (float)(((4 * g + j) * (c >> 1)) & 15) * (1.f / 16); cs[j] = cosr(a); sn[j] = sinr(a); }
    const u32 c0 = pk(cs[0], cs[1]), c1 = pk(cs[2], cs[3]), s0 = pk(sn[0], sn[1]), s1_ = pk(sn[2], sn[3]);
    Am = (c & 1) ? u32x4{s0 ^ SGN_BOTH, s1_ ^ SGN_BOTH, c0, c1} : u32x4{c0, c1, s0, s1_};
#pragma unroll
    for (int q = 0; q < 2; ++q) {
      const float a = (float)(((c >> 1) * (s1 + 256 * (2 * g + q))) & 2047) * (1.f / 2048), cc = cosr(a), ss = sinr(a);
      G[q] = (c & 1) ? pk(-ss, cc) : pk(cc, ss);
    }
  }
  PIN(Bc); PIN(Bs); PIN(Am); PIN(G); SB();
  if (POLL) { xd = poll4(rw, xo, xs, cap); STAMP(10); }
  char* tr = scr + 16384 + w * 1280;
  *reinterpret_cast<u32x4*>(tr + g * 288 + c * 16) = xd;
  float v[8], s = 0.f;
#pragma unroll
  for (int p = 0; p < 4; ++p) s += blo(xd[p]) + bhi(xd[p]);
  __builtin_amdgcn_fence(__ATOMIC_RELEASE, "wavefront"); __builtin_amdgcn_wave_barrier(); __builtin_amdgcn_fence(__ATOMIC_ACQUIRE, "wavefront");
#pragma unroll
  for (int j = 0; j < 8; ++j) v[j] = blo(*reinterpret_cast<const u16*>(tr + g * 288 + j * 32 + c * 2));
  const float mean = wave_sum(s) * (1.f / 512);
  float q = 0.f;
#pragma unroll
  for (int j = 0; j < 8; ++j) { v[j] -= mean; q += v[j] * v[j]; }
  const float rs = __builtin_amdgcn_rsqf(wave_sum(q) * (1.f / 512) + 1e-5f);
  const u32x4 a = {pk(v[0] * rs, v[1] * rs), pk(v[2] * rs, v[3] * rs), pk(v[4] * rs, v[5] * rs), pk(v[6] * rs, v[7] * rs)};
  const u32 ny = c == 0 ? 0xBF803F80u : 0u;
  u32* L = reinterpret_cast<u32*>(scr);
#pragma unroll
  for (int t = 0; t < 2; ++t) {
    const u32 f = t ? SGN_HI : 0u;
    const f32x4 dr = mma32(a, Bc ^ f, Z4), di = mma32(a, Bs ^ f, Z4);
    f32x4 re, im;
#pragma unroll
    for (int i = 0; i < 4; ++i) { re[i] = dr[i] * tc[t][i] + di[i] * ts[t][i]; im[i] = di[i] * tc[t][i] - dr[i] * ts[t][i]; }
    const u32x4 b = packc(re, im);
    f32x4 R = mma32(Am, b, Z4);
    if (t == 0) { const f32x4 n = mma32(u32x4{ny, ny, 0u, 0u}, b, Z4); if (l == 0) { R[0] = n[0]; R[1] = 0.f; } }
#pragma unroll
    for (int q2 = 0; q2 < 2; ++q2) L[((w >> 1) * 264 + c + 16 * t + 32 * (2 * g + q2)) * 2 + (w & 1)] = pk(R[2 * q2], R[2 * q2 + 1]);
  }
  asm volatile("s_waitcnt lgkmcnt(0)" ::: "memory"); __builtin_amdgcn_s_barrier(); asm volatile("" ::: "memory");
  const f32x4 o0 = mma16(G, reinterpret_cast<const u32x2*>(L)[g * 264 + c + 32 * w], Z4), o1 = mma16(G, reinterpret_cast<const u32x2*>(L)[g * 264 + c + 32 * w + 16], Z4);
  st4<(POLL || ZT) ? 16 : 0>(swp(u32x4{san(pk(o0[0], o0[1])), san(pk(o0[2], o0[3])), san(pk(o1[0], o1[1])), san(pk(o1[2], o1[3]))}), rw,
                     ((c + 32 * w + 16 * (g & 1)) * 256 + s1) * 32 + (g & 2) * 8, zs);
}

__global__ __launch_bounds__(768) void k_pre(const u16* __restrict__ x, const u16* __restrict__ wr, const u16* __restrict__ wi, const u16* __restrict__ wg,
                                                const u16* __restrict__ wu, const u16* __restrict__ wd, char* __restrict__ ws DARG) {
  __shared__ __attribute__((aligned(16))) char scr[16384 + 8 * 1280 + 256];
  u32 stv = 0; (void)stv;
  const int b = blockIdx.x, tid = threadIdx.x, w = __builtin_amdgcn_readfirstlane(tid >> 6);
  STAMP(0);
  if (w >= 8) {
    if (w == 11) {
#pragma unroll
      for (int i = 0; i < 8; ++i) st4<16>(u32x4{SENT, SENT, SENT, SENT}, mkrs(ws, WS_END), (tid & 63) * 16, O_Y1 + (b * 8 + i) * 1024);
    }
    if (w < 11 || b < 8) pre_tile(((w - 8) * 256 + b) * 64 + (tid & 63), wr, wi, wg, wu, wd, ws);
    else if (b < 24) {
      const char *c0, *c1;
      asm volatile("s_getpc_b64 vcc\n\ts_add_u32 vcc_lo, vcc_lo, k_main@rel32@lo+4\n\ts_addc_u32 vcc_hi, vcc_hi, k_main@rel32@hi+12\n\ts_mov_b64 %0, vcc" : "=s"(c0) :: "vcc", "scc");
      asm volatile("s_getpc_b64 vcc\n\ts_add_u32 vcc_lo, vcc_lo, k_end@rel32@lo+4\n\ts_addc_u32 vcc_hi, vcc_hi, k_end@rel32@hi+12\n\ts_mov_b64 %0, vcc" : "=s"(c1) :: "vcc", "scc");
      const long n = c1 - c0;
      for (long o = (tid & 63) * 128; n < 65536 && o < n; o += 8192) { u32 d; asm volatile("global_load_dword %0, %1, off\n\ts_waitcnt vmcnt(0)" : "=v"(d) : "v"(c0 + o) : "memory"); }
    }
    return;
  }
  const rsrc_t rw = mkrs(ws, WS_END), rx = mkrs(x, 2 * MB);
  if (tid < 4) *reinterpret_cast<u32*>(ws + O_CNT + (b * 4 + tid) * 256) = 0u;
  int cap = 0;
  const int s1 = (b & 7) * 32 + (b >> 3);
  const u32x4 xd = ld4<0>(rx, (s1 + 256 * w) * 1024 + (tid & 63) * 16, 0);
  phase_a1<false>(xd, 0, rw, O_Z1, scr, s1, w, tid & 63, cap SPASS);
  STAMP(1); VW(); STAMP(4); FLUSH(19);
}

DEV void glds16(const void* gsrc, u32 lds_dst) {
  u32 keep;
  asm volatile("s_mov_b32 %0, m0\n\ts_mov_b32 m0, %2\n\ts_nop 0\n\tglobal_load_lds_dwordx4 %1, off\n\ts_mov_b32 m0, %0" : "=&s"(keep) : "v"(gsrc), "s"(lds_dst) : "memory");
}
#define XW 10
#define XA 16
#define PRIO 0
#define HW 7
#define WD0 8

extern "C" __global__ __launch_bounds__(512, 2) void k_main(const u16* __restrict__ x, char* __restrict__ ws, u16* __restrict__ out DARG) {
  extern __shared__ __attribute__((aligned(16))) u32x4 SM[];
  char* scr = reinterpret_cast<char*>(SM) + 98304;
  u32 stv = 0; (void)stv;
  const int b = blockIdx.x, tid0 = threadIdx.x, w0 = __builtin_amdgcn_readfirstlane(tid0 >> 6);
  const int grp = b & 7, r_ = b >> 3, s1 = grp * 32 + r_;
  const rsrc_t rw = mkrs(ws, WS_END), rx0 = mkrs(x, 2 * MB), ro = mkrs(out, 2 * MB);
  int cap = SPIN_CAP;
  STAMP(0);
  u32x4 wt, zd;
  asm volatile("global_load_dwordx4 %0, %2, off\n\tglobal_load_dwordx4 %1, %3, off" : "=&v"(wt), "=&v"(zd)
               : "v"(ws + O_WT + s1 * 8192 + tid0 * 16), "v"(ws + O_Z1 + s1 * 8192 + tid0 * 16) : "memory");
  st4<16>(u32x4{SENT, SENT, SENT, SENT}, rw, tid0 * 16, O_Z2 + s1 * 8192);
  const int NG = r_ & 7, TG = grp * 4 + (r_ >> 3), F0 = r_ * 3, rot0 = (r_ * 10) & 30, rot20 = r_ < 29 ? (NG * 11) >> 3 : 0;
#define FSTEP(s) (11 * w + (s) + rot2 - ((s) + rot2 >= 11 ? 11 : 0))
#define LDWD1(s, h) asm volatile("s_cmp_lg_u32 %4, 0\n\ts_cbranch_scc1 1f\n\tbuffer_load_dwordx4 %0, %1, %2, %3 offen\n1:" : "+v"(wd[s][h]) \
    : "v"(wdl), "s"(rw), "s"(O_WD + ((2 * NG + (h)) * 88 + FSTEP(s)) * 1024), "s"(L) : "scc")
#define LDWDALL(h) { u32 o_ = O_WD + ((2 * NG + (h)) * 88 + 11 * w) * 1024; asm volatile("s_cmp_lg_u32 %14, 0\n\ts_cbranch_scc1 1f\n\t" \
    "buffer_load_dwordx4 %0, %12, %13, %11 offen\n\ts_add_u32 %11, %11, 1024\n\tbuffer_load_dwordx4 %1, %12, %13, %11 offen\n\ts_add_u32 %11, %11, 1024\n\tbuffer_load_dwordx4 %2, %12, %13, %11 offen\n\ts_add_u32 %11, %11, 1024\n\tbuffer_load_dwordx4 %3, %12, %13, %11 offen\n\ts_add_u32 %11, %11, 1024\n\tbuffer_load_dwordx4 %4, %12, %13, %11 offen\n\ts_add_u32 %11, %11, 1024\n\tbuffer_load_dwordx4 %5, %12, %13, %11 offen\n\ts_add_u32 %11, %11, 1024\n\tbuffer_load_dwordx4 %6, %12, %13, %11 offen\n\ts_add_u32 %11, %11, 1024\n\tbuffer_load_dwordx4 %7, %12, %13, %11 offen\n\ts_add_u32 %11, %11, 1024\n\tbuffer_load_dwordx4 %8, %12, %13, %11 offen\n\ts_add_u32 %11, %11, 1024\n\tbuffer_load_dwordx4 %9, %12, %13, %11 offen\n\ts_add_u32 %11, %11, 1024\n\tbuffer_load_dwordx4 %10, %12, %13, %11 offen\n\ts_add_u32 %11, %11, 1024\n\ts_waitcnt vmcnt(0)\n1:" \
    : "+v"(wd[0][h]), "+v"(wd[1][h]), "+v"(wd[2][h]), "+v"(wd[3][h]), "+v"(wd[4][h]), "+v"(wd[5][h]), "+v"(wd[6][h]), "+v"(wd[7][h]), "+v"(wd[8][h]), "+v"(wd[9][h]), "+v"(wd[10][h]), "+s"(o_) : "v"(wdl), "s"(rw), "s"(L) : "scc"); }
  const u32 lb = (u32)(size_t)(__attribute__((address_space(3))) u32x4*)SM;
#define LDGU(i) { const char* src = ws + O_WG + (min(F0 + (i), NF - 1) * 32 + w * 4) * 1024 + l * 16; \
    _Pragma("unroll") for (int j = 0; j < 4; ++j) glds16(src + j * 1024, lb + (((4 * w + j - rot) & 31) * 3 + (i)) * 1024); }

  u32x4 wd[11][2];
  for (int L = 0; L < 2; ++L) {
    {
      LANE();

      float cs[4], sn[4], tc[4], ts[4];
#pragma unroll
      for (int j = 0; j < 4; ++j) {
        const int p = (4 * g + j) * c;
        const float a = (float)(p & 15) * (1.f / 16), bb = (float)p * (1.f / 256);
        cs[j] = cosr(a); sn[j] = sinr(a); tc[j] = cosr(bb); ts[j] = sinr(bb); PIN(tc[j]); PIN(ts[j]);
      }
      u32x4 B1r = {pk(cs[0], sn[0]), pk(cs[1], sn[1]), pk(cs[2], sn[2]), pk(cs[3], sn[3])};
      u32x4 B1i = {pk(-sn[0], cs[0]), pk(-sn[1], cs[1]), pk(-sn[2], cs[2]), pk(-sn[3], cs[3])};
      u32x4 Fr = {pk(cs[0], cs[1]), pk(cs[2], cs[3]), pk(sn[0], sn[1]), pk(sn[2], sn[3])};
      PIN(B1r); PIN(B1i); PIN(Fr); SB();
      if (L == 1) zd = poll4(rw, tid * 16, O_Z2 + s1 * 8192, cap);
      asm volatile("s_waitcnt vmcnt(1)" : "+v"(wt), "+v"(zd) :: "memory");
      u32* P = reinterpret_cast<u32*>(scr + 8448);
#pragma unroll
      for (int i = 0; i < 4; ++i) { const int sa = tid >> 1; P[(4 * (tid & 1) + i) * 312 + sa + 16 * (sa >> 6)] = zd[i]; }
      if (L == 0) LDGU(0)
      asm volatile("s_waitcnt lgkmcnt(0)" ::: "memory"); __builtin_amdgcn_s_barrier(); asm volatile("" ::: "memory");
      STAMP(1 + 12 * L);
      u32x4 a;
#pragma unroll
      for (int j = 0; j < 4; ++j) a[j] = P[w * 312 + 80 * g + 16 * j + c];
      const u32x4 Fi = {Fr[2] ^ SGN_BOTH, Fr[3] ^ SGN_BOTH, Fr[0], Fr[1]}, Ir = {Fr[0], Fr[1], Fi[0], Fi[1]}, Ii = {Fr[2], Fr[3], Fr[0], Fr[1]};
      f32x4 dr = mma32(a, B1r, Z4), di = mma32(a, B1i, Z4), re, im;
#pragma unroll
      for (int i = 0; i < 4; ++i) { re[i] = dr[i] * tc[i] + di[i] * ts[i]; im[i] = di[i] * tc[i] - dr[i] * ts[i]; }
      u32x4 bb = packc(re, im);
      dr = mma32(Fr, bb, Z4); di = mma32(Fi, bb, Z4);
#pragma unroll
      for (int i = 0; i < 4; ++i) { const float wr = blo(wt[i]), wi = bhi(wt[i]); re[i] = dr[i] * wr - di[i] * wi; im[i] = di[i] * wr + dr[i] * wi; }
      bb = packc(re, im);
      dr = mma32(bb, Ir, Z4); di = mma32(bb, Ii, Z4);
#pragma unroll
      for (int i = 0; i < 4; ++i) { re[i] = dr[i] * tc[i] - di[i] * ts[i]; im[i] = di[i] * tc[i] + dr[i] * ts[i]; }
      bb = packc(re, im);
      dr = mma32(Ir, bb, Z4); di = mma32(Ii, bb, Z4);
      u32* Q = reinterpret_cast<u32*>(scr + 20480);
#pragma unroll
      for (int i = 0; i < 4; ++i) Q[(16 * (4 * g + i) + c) * 8 + (w ^ (((c >> 2) & 1) * 4))] = san(pk(dr[i], di[i]));
      asm volatile("s_waitcnt vmcnt(4) lgkmcnt(0)" ::: "memory");   __builtin_amdgcn_s_barrier(); asm volatile("" ::: "memory");
      st4<16>(reinterpret_cast<const u32x4*>(Q)[tid ^ ((tid >> 3) & 1)], rw, ((tid >> 1) * 256 + s1) * 32 + (tid & 1) * 16, L ? O_Y2 : O_Y1);
      STAMP(2 + 12 * L);
    }
    {
      LANE();
      const u32 xo = (s1 + 256 * w) * 1024 + 32 * c + 8 * (g & 2) + 512 * (g & 1);
      u32x4 xr;
      if (L == 0) xr = ld4<0>(rx0, xo, 0); else xr = ld4<16>(rw, xo, O_X2);
      u32x2 G, Br, Bi; u32x4 B2; float tc[2][4], ts[2][4];
#pragma unroll
      for (int q = 0; q < 2; ++q) {
        const float a = (float)(((2 * g + q) * (s1 + 256 * (c >> 1))) & 2047) * (1.f / 2048), cc = cosr(a), ss = sinr(a);
        G[q] = (c & 1) ? pk(ss, cc) : pk(cc, -ss);
        const float bb = (float)(((2 * g + q) * c) & 15) * (1.f / 16), cb = cosr(bb), sb = sinr(bb);
        Br[q] = pk(cb, -sb); Bi[q] = pk(sb, cb);
      }
#pragma unroll
      for (int t = 0; t < 2; ++t)
#pragma unroll
        for (int i = 0; i < 4; ++i) { const float a = (float)((16 * t + 4 * g + i) * c) * (1.f / 512); tc[t][i] = cosr(a); ts[t][i] = sinr(a); PIN(tc[t][i]); PIN(ts[t][i]); }
      {
        float cs[4], sn[4];
#pragma unroll
        for (int j = 0; j < 4; ++j) { const float a = (float)(((4 * g + j) * c) & 31) * (1.f / 32); cs[j] = cosr(a); sn[j] = -sinr(a); }
        B2 = u32x4{pk(cs[0], cs[1]), pk(cs[2], cs[3]), pk(sn[0], sn[1]), pk(sn[2], sn[3])};
      }
      PIN(G); PIN(Br); PIN(Bi); PIN(B2); SB();
      const u32x4 y = swp(poll4(rw, (s1 * 256 + c + 32 * w + 16 * (g & 1)) * 32 + (g & 2) * 8, L ? O_Y2 : O_Y1, cap));
      if (L == 0) {
        st4<16>(u32x4{SENT, SENT, SENT, SENT}, rw, tid * 16, O_Y2 + s1 * 8192);
        st4<16>(u32x4{SENT, SENT, SENT, SENT}, rw, l * 16, O_X2 + (s1 + 256 * w) * 1024);
        LDGU(1) LDGU(2)
      }
      STAMP(3 + 12 * L);
      u32x2* Lp = reinterpret_cast<u32x2*>(scr);
#pragma unroll
      for (int u = 0; u < 2; ++u) { const f32x4 o = mma16(G, u32x2{y[2 * u], y[2 * u + 1]}, Z4); Lp[g * 264 + c + 16 * (2 * w + u)] = u32x2{pk(o[0], o[1]), pk(o[2], o[3])}; }
      asm volatile("s_waitcnt lgkmcnt(0)" ::: "memory"); __builtin_amdgcn_s_barrier(); asm volatile("" ::: "memory");
      u32 r[2][2];
#pragma unroll
      for (int t = 0; t < 2; ++t)
#pragma unroll
        for (int q = 0; q < 2; ++q) r[t][q] = reinterpret_cast<const u32*>(scr)[((w >> 1) * 264 + c + 16 * t + 32 * (2 * g + q)) * 2 + (w & 1)];
      const float nyq = blo(__builtin_amdgcn_readfirstlane(r[0][0]));
      if (l == 0) r[0][0] = 0u;
      f32x4 acc[2];
      xr = swp(xr);
#pragma unroll
      for (int t = 0; t < 2; ++t) acc[t] = f32x4{blo(xr[2 * t]), bhi(xr[2 * t]), blo(xr[2 * t + 1]), bhi(xr[2 * t + 1])};
#pragma unroll
      for (int t = 0; t < 2; ++t) {
        const u32x2 a = {r[t][0], r[t][1]};
        f32x4 dr = mma16(a, Br, Z4), di = mma16(a, Bi, Z4), re, im;
        if (t == 0) dr[0] += g == 0 ? ((c & 1) ? -nyq : nyq) : 0.f;
#pragma unroll
        for (int i = 0; i < 4; ++i) { re[i] = dr[i] * tc[t][i] - di[i] * ts[t][i]; im[i] = di[i] * tc[t][i] + dr[i] * ts[t][i]; }
        const u32x4 a2 = packc(re, im);
        const u32 f = (t && (c & 1)) ? SGN_BOTH : 0u;
        acc[0] = mma32(a2, B2 ^ f, acc[0]); acc[1] = mma32(a2, B2 ^ (f ^ SGN_HI), acc[1]);
      }
      const u32x4 xq = packc(acc[0], acc[1]);
      st4<16>(swp(xq), rw, xo, O_X1 + L * 2 * MB);
      float v[8], s = 0.f;
#pragma unroll
      for (int p = 0; p < 4; ++p) { v[2 * p] = blo(xq[p]); v[2 * p + 1] = bhi(xq[p]); s += v[2 * p] + v[2 * p + 1]; }
      const float mean = wave_sum(s) * (1.f / 512);
      float q = 0.f;
#pragma unroll
      for (int j = 0; j < 8; ++j) { v[j] -= mean; q += v[j] * v[j]; }
      const float rs = __builtin_amdgcn_rsqf(wave_sum(q) * (1.f / 512) + 1e-5f);
      u32x4* Xt = reinterpret_cast<u32x4*>(scr + 8448);
      Xt[l * 8 + (w ^ (l & 7))] = u32x4{pk(v[0] * rs, v[1] * rs), pk(v[2] * rs, v[3] * rs), pk(v[4] * rs, v[5] * rs), pk(v[6] * rs, v[7] * rs)};
      asm volatile("s_waitcnt lgkmcnt(0)" ::: "memory"); __builtin_amdgcn_s_barrier(); asm volatile("" ::: "memory");
      st4<16>(Xt[tid ^ ((tid >> 3) & 7)], rw, ((s1 >> 2) * 2048 + (tid >> 3) * 32 + (s1 & 3) * 8 + (tid & 7)) * 16, O_XN + L * 2 * MB);
      STAMP(4 + 12 * L);
    }
    gu32* cnt = (gu32*)(ws + O_CNT + (L * 16 + grp) * 8192);
    arrive(cnt + (r_ & (NSH - 1)) * 64); STAMP(24 + 4 * L);
    LANE();
    const u32 wdl = l * 16;
    if (r_ < 29) {
      await(cnt, 31, cap, w, l);
      STAMP(5 + 12 * L);
      const u32 xo = (grp * 8 + w) * 32768 + g * 512 + c * 16, xs = O_XN + L * 2 * MB;
#define LDX(n) xf[n] = ld4<16>(rw, xo + ((n) & 1) * 256, xs + ((((n) & 30) + rot) & 31) * 1024)
      u32x4 xf[32], wf[2][3];
#pragma unroll
      for (int i = 0; i < XW; ++i) LDX(i);
#pragma unroll
      for (int i = 0; i < 3; ++i) wf[0][i] = SM[i * 64 + l];
      f32x4 acc[2][3][2] = {};
      if (w < 4) __builtin_amdgcn_s_setprio(PRIO);
      SB();
#define B1LOOP(W) _Pragma("unroll") for (int ks = 0; ks < 32; ++ks) { \
        if (ks + XW < 32) LDX(ks + XW); \
        if (ks >= WD0 && ks < WD0 + 22) LDWD1((ks - WD0) >> 1, (ks - WD0) & 1); \
        if (ks + 1 < 32) { _Pragma("unroll") for (int i = 0; i < 3; ++i) wf[(ks + 1) & 1][i] = SM[((ks + 1) * 3 + i) * 64 + l]; } \
        SB(); \
        _Pragma("unroll") for (int i = 0; i < 6; ++i) acc[ks & 1][i >> 1][i & 1] = mma32(wf[ks & 1][i >> 1], xf[(ks & 30) + (i & 1)], acc[ks & 1][i >> 1][i & 1]); \
        SB(); }
      B1LOOP(1)
#pragma unroll
      for (int i = 0; i < 3; ++i) {
        float h[8];
#pragma unroll
        for (int r = 0; r < 8; ++r) h[r] = fmaxf(acc[0][i][r >> 2][r & 3], 0.f) * acc[1][i][r >> 2][r & 3];
        if (F0 + i < NF) st4<16>(swp(u32x4{pk(h[0], h[1]), pk(h[2], h[3]), pk(h[4], h[5]), pk(h[6], h[7])}), rw, ((grp * 8 + w) * NF + F0 + i) * 1024 + l * 16, O_H + L * 6 * MB);
      }
      __builtin_amdgcn_s_setprio(0);
      STAMP(6 + 12 * L);
      arrive(cnt + 8 * 2048 + (r_ & (NSH - 1)) * 64); STAMP(25 + 4 * L);
    }
    else { LDWDALL(0) LDWDALL(1) }
    {
      await(cnt + 8 * 2048, 28, cap, w, l);
      STAMP(7 + 12 * L);
      const u32 hs = O_H + L * 6 * MB + 2 * TG * NF * 1024;
      const u32 oo = (8 * TG + w + 256 * (l >> 3)) * 1024 + NG * 128 + (l & 7) * 16;
      u32x4 hf[11][2];
#define LDH(s) { const u32 o_ = hs + min(FSTEP(s), NF - 1) * 1024; hf[s][0] = ld4<16>(rw, l * 16, o_); hf[s][1] = ld4<16>(rw, l * 16, o_ + NF * 1024); }
#pragma unroll
      for (int s = 0; s < HW; ++s) LDH(s)
      f32x16 acc[4] = {};
      u32x4 res;
      SB();
#pragma unroll
      for (int s = 0; s < 11; ++s) {
        if (s + HW < 11) LDH(s + HW)
        if (s + HW == 11) res = ld4<16>(rw, oo, O_X1 + L * 2 * MB);
        SB();
#pragma unroll
        for (int q = 0; q < 4; ++q) acc[q] = mmb(wd[s][q & 1], hf[s][q >> 1], acc[q]);
        SB();
      }
      STAMP(8 + 12 * L);
      u32x4* Rd = reinterpret_cast<u32x4*>(scr);
#pragma unroll
      for (int d = 0; d < 8; ++d) {
        const f32x16 A = acc[d >> 1]; const int o = 8 * (d & 1);
        Rd[w * 512 + (32 * (d >> 2) + (l & 31)) * 8 + ((4 * ((d >> 1) & 1) + 2 * (l >> 5) + (d & 1)) ^ (l & 7))] =
            u32x4{pk(A[o], A[o + 1]), pk(A[o + 2], A[o + 3]), pk(A[o + 4], A[o + 5]), pk(A[o + 6], A[o + 7])};
      }
      asm volatile("s_waitcnt lgkmcnt(0)" ::: "memory"); __builtin_amdgcn_s_barrier(); asm volatile("" ::: "memory");
      STAMP(26 + 4 * L);
      float own[8] = {};
#pragma unroll
      for (int j = 0; j < 8; ++j) {
        const u32x4 p = Rd[j * 512 + w * 64 + (l ^ (l >> 3))];
#pragma unroll
        for (int i = 0; i < 4; ++i) { own[2 * i] += blo(p[i]); own[2 * i + 1] += bhi(p[i]); }
      }
      u32x4 o;
#pragma unroll
      for (int i = 0; i < 4; ++i) o[i] = pk(own[2 * i] + blo(res[i]), own[2 * i + 1] + bhi(res[i]));
      if (L == 1) { st4<0>(o, ro, oo, 0); STAMP(21); VW(); STAMP(22); FLUSH(r_ < 29 ? 0x77FFEFFFu : 0x557FEFFFu & ~0x60060u); break; }
      st4<16>(u32x4{san(o[0]), san(o[1]), san(o[2]), san(o[3])}, rw, oo, O_X2);
      asm volatile("s_waitcnt lgkmcnt(0)" ::: "memory"); __builtin_amdgcn_s_barrier(); asm volatile("" ::: "memory");
      STAMP(9);
    }
    { LANE(); phase_a1<true>(zd, O_X2, rw, O_Z2, scr, s1, w, l, cap SPASS); STAMP(11); }
  }
}

extern "C" __global__ void k_end() {}

extern "C" void kernel_launch(void* const* d_in, const int* in_sizes, int n_in, void* d_out, int out_size,
                              void* d_ws, size_t ws_size, hipStream_t stream) {
  (void)hipFuncSetAttribute((const void*)k_main, hipFuncAttributeMaxDynamicSharedMemorySize, 163840);
  k_pre<<<256, 768, 0, stream>>>((const u16*)d_in[0], (const u16*)d_in[1], (const u16*)d_in[2], (const u16*)d_in[4], (const u16*)d_in[5],
                                       (const u16*)d_in[6], (char*)d_ws DPASS(0));
  k_main<<<256, 512, 163840, stream>>>((const u16*)d_in[0], (char*)d_ws, (u16*)d_out DPASS(1));
}
```

```cpp
#include <hip/hip_runtime.h>
#include <hip/hip_bf16.h>

typedef unsigned short u16;
typedef unsigned int u32;
typedef float f32x2 __attribute__((ext_vector_type(2)));
typedef float f32x4 __attribute__((ext_vector_type(4)));
typedef float f32x16 __attribute__((ext_vector_type(16)));
typedef u32 u32x2 __attribute__((ext_vector_type(2)));
typedef u32 u32x4 __attribute__((ext_vector_type(4)));
typedef __bf16 bf2 __attribute__((ext_vector_type(2)));
typedef __bf16 bf8 __attribute__((ext_vector_type(8)));
typedef short s16x4 __attribute__((ext_vector_type(4)));

#define DEV __device__ __forceinline__
#define DARG
#define DPASS(n)
#define STAMP(i)
#define FLUSH(m)
#define SARG
#define SPASS
#define VW()
constexpr int NF = 86;
constexpr u32 SGN_HI = 0x80000000u, SGN_LO = 0x00008000u, SGN_BOTH = 0x80008000u;

DEV u32 pk(float a, float b) { f32x2 v = {a, b}; return __builtin_bit_cast(u32, __builtin_convertvector(v, bf2)); }
DEV float blo(u32 v) { return __builtin_bit_cast(float, v << 16); }
DEV float bhi(u32 v) { return __builtin_bit_cast(float, v & 0xffff0000u); }
DEV float cosr(float r) { return __builtin_amdgcn_cosf(r); }
DEV float sinr(float r) { return __builtin_amdgcn_sinf(r); }
DEV f32x4 mma32(u32x4 a, u32x4 b, f32x4 c) { return __builtin_amdgcn_mfma_f32_16x16x32_bf16(__builtin_bit_cast(bf8, a), __builtin_bit_cast(bf8, b), c, 0, 0, 0); }
DEV f32x4 mma16(u32x2 a, u32x2 b, f32x4 c) { return __builtin_amdgcn_mfma_f32_16x16x16bf16_1k(__builtin_bit_cast(s16x4, a), __builtin_bit_cast(s16x4, b), c, 0, 0, 0); }
DEV f32x16 mmb(u32x4 a, u32x4 b, f32x16 c) { return __builtin_amdgcn_mfma_f32_32x32x16_bf16(__builtin_bit_cast(bf8, a), __builtin_bit_cast(bf8, b), c, 0, 0, 0); }
template <int CTRL> DEV float dpp(float v) { return __builtin_bit_cast(float, __builtin_amdgcn_update_dpp(0, __builtin_bit_cast(int, v), CTRL, 0xf, 0xf, true)); }
DEV float wave_sum(float v) {
  v += dpp<0xB1>(v); v += dpp<0x4E>(v); v += dpp<0x141>(v); v += dpp<0x140>(v);
  u32 u = __builtin_bit_cast(u32, v);
  auto r = __builtin_amdgcn_permlane16_swap(u, u, false, false);
  const u32 r0 = r[0], r1 = r[1];
  v = __builtin_bit_cast(float, r0) + __builtin_bit_cast(float, r1);
  u = __builtin_bit_cast(u32, v);
  auto q = __builtin_amdgcn_permlane32_swap(u, u, false, false);
  const u32 q0 = q[0], q1 = q[1];
  return __builtin_bit_cast(float, q0) + __builtin_bit_cast(float, q1);
}
DEV u32x4 packc(const f32x4& re, const f32x4& im) { return u32x4{pk(re[0], re[1]), pk(re[2], re[3]), pk(im[0], im[1]), pk(im[2], im[3])}; }
const f32x4 __device__ Z4 = {0.f, 0.f, 0.f, 0.f};

struct Item { const u16* src; long A; int B, C; float sc; u32x4* dst; };
DEV Item pre_item(int it, int l, const u16* wr, const u16* wi, const u16* wg, const u16* wu, const u16* wd, u32x4* WT, u32x4* WG, u32x4* WD) {
  if (it < 2048) {
    const int pb = it >> 2, r = pb >> 3, m2 = r >> 3, g = (r >> 1) & 3, c = 8 * (r & 1) + (l & 7);
    const int k = 32 * (pb & 7) + 8 * (it & 3) + (l >> 3);
    return {wr + (m2 + 8 * (c + 64 * g)) * 257 + (k ? k : 256), wi - wr, 128 * 257, 256 * 257, k ? 0x1p-19f : 0x1p-20f, WT + (k * 8 + m2) * 64 + 16 * g + c};
  }
  if (it < 2048 + 2752) {
    const int q = it - 2048, F = q >> 5, r = l & 31, lam = 2 * (q & 31) + (l >> 5), d0 = 16 * (lam & 15) + 4 * (lam >> 4);
    return {(r < 16 ? wg : wu) + 16 * F + (r & 15) + d0 * 1376, 1376, 2752, 256 * 1376, 1.f, WG + q * 64 + l};
  }
  const int q = min(it - 4800, 1407), nt = q & 15, F = q >> 4, r = l & 31;
  const int n = 32 * nt + 16 * ((r >> 2) & 1) + (r & 3) + 4 * (r >> 3);
  return {wd + (16 * min(F, NF - 1) + 4 * (l >> 5)) * 512 + n, 512, 1024, 4096, F < NF ? 1.f : 0.f, WD + (nt * 88 + F) * 64 + l};
}

constexpr u32 MB = 1u << 20;
constexpr u32 O_Z1 = 0, O_Y1 = 2 * MB, O_Z2 = 4 * MB, O_Y2 = 6 * MB, O_X2 = 8 * MB, O_WT = 10 * MB, O_X1 = 12 * MB  ,
              O_XN = 16 * MB  , O_WG = 20 * MB, O_WD = 23 * MB, O_CNT = 25 * MB, O_H = 26 * MB  , WS_END = 38 * MB;
constexpr u32 SENT = 0xFFFFFFFFu;
constexpr int SPIN_CAP = 1 << 18;
typedef __amdgpu_buffer_rsrc_t rsrc_t;
typedef __attribute__((address_space(1))) u32 gu32;
DEV rsrc_t mkrs(const void* p, u32 n) { return __builtin_amdgcn_make_buffer_rsrc(const_cast<void*>(p), 0, n, 0x00020000); }
template <int AUX> DEV u32x4 ld4(rsrc_t r, u32 v, u32 s) { return __builtin_amdgcn_raw_buffer_load_b128(r, v, s, AUX); }
template <int AUX> DEV u32x2 ld2(rsrc_t r, u32 v, u32 s) { return __builtin_amdgcn_raw_buffer_load_b64(r, v, s, AUX); }
template <int AUX> DEV void st4(u32x4 d, rsrc_t r, u32 v, u32 s) { __builtin_amdgcn_raw_buffer_store_b128(d, r, v, s, AUX); }
template <int AUX> DEV void st2(u32x2 d, rsrc_t r, u32 v, u32 s) { __builtin_amdgcn_raw_buffer_store_b64(d, r, v, s, AUX); }
DEV u32x4 swp(u32x4 v) {
  auto p = __builtin_amdgcn_permlane16_swap(v[0], v[2], false, false); const u32 p0 = p[0], p1 = p[1];
  auto q = __builtin_amdgcn_permlane16_swap(v[1], v[3], false, false); const u32 q0 = q[0], q1 = q[1];
  return u32x4{p0, q0, p1, q1};
}
#define PSL 0
DEV u32 san(u32 v) { return min(v, SENT - 1u); }
DEV u32 mx4(u32x4 v) { return max(max(v[0], v[1]), max(v[2], v[3])); }
#define PIN(x) asm volatile("" : "+v"(x))
#define LANE() int l = tid0 & 63, w = w0, rot = rot0, rot2 = rot20; PIN(l); asm volatile("" : "+s"(w), "+s"(rot), "+s"(rot2)); const int c = l & 15, g = l >> 4, tid = w * 64 + l; (void)c; (void)g; (void)tid
#define SB() __builtin_amdgcn_sched_barrier(0)
#define DRAIN() asm volatile("s_waitcnt vmcnt(0)" ::: "memory")
DEV u32x4 poll4(rsrc_t r, u32 v, u32 s, int& cap) {
  u32x4 d; int n = 0;
  for (;;) { asm volatile("" ::: "memory"); d = ld4<16>(r, v, s); if (!__any(mx4(d) == SENT) || ++n >= cap) break; if (PSL) __builtin_amdgcn_s_sleep(PSL); }
  if (n >= cap) cap = 0;
  return d;
}
#define NSH 32
DEV void arrive(gu32* c) { DRAIN(); __syncthreads(); if (threadIdx.x == 0) __hip_atomic_fetch_add(c, 1u, __ATOMIC_RELAXED, __HIP_MEMORY_SCOPE_AGENT); }
DEV void await(gu32* c, int last, int& cap, int w, int l) {
  if (w == 0) {
    const int i = l & (NSH - 1); const u32 n = (last - i + NSH) / NSH; int k = 0;
    while (__any(__hip_atomic_load(c + i * 64, __ATOMIC_RELAXED, __HIP_MEMORY_SCOPE_AGENT) < n) && ++k < cap) __builtin_amdgcn_s_sleep(1);
    if (k >= cap) cap = 0;
  }
  __syncthreads();
}

template <bool POLL> DEV void phase_a1(rsrc_t rx, u32 xs, rsrc_t rw, u32 zs, char* scr, int s1, int w, int l, int& cap SARG) {
  const int c = l & 15, g = l >> 4;
  const u32 xo = (s1 + 256 * w) * 1024 + l * 16;
  u32x4 xd;
  if (!POLL) xd = ld4<0>(rx, xo, xs);
  u32x4 Bc, Bs, Am; u32x2 G; float tc[2][4], ts[2][4];
#pragma unroll
  for (int p = 0; p < 4; ++p) {
    const float a0 = (float)(((8 * g + 2 * p) * c) & 31) * (1.f / 32), a1 = (float)(((8 * g + 2 * p + 1) * c) & 31) * (1.f / 32);
    Bc[p] = pk(cosr(a0), cosr(a1)); Bs[p] = pk(-sinr(a0), -sinr(a1));
  }
#pragma unroll
  for (int t = 0; t < 2; ++t)
#pragma unroll
    for (int i = 0; i < 4; ++i) { const float a = (float)((c + 16 * t) * (4 * g + i)) * (1.f / 512); tc[t][i] = cosr(a); ts[t][i] = sinr(a); PIN(tc[t][i]); PIN(ts[t][i]); }
  {
    float cs[4], sn[4];
#pragma unroll
    for (int j = 0; j < 4; ++j) { const float a = (float)(((4 * g + j) * (c >> 1)) & 15) * (1.f / 16); cs[j] = cosr(a); sn[j] = sinr(a); }
    const u32 c0 = pk(cs[0], cs[1]), c1 = pk(cs[2], cs[3]), s0 = pk(sn[0], sn[1]), s1_ = pk(sn[2], sn[3]);
    Am = (c & 1) ? u32x4{s0 ^ SGN_BOTH, s1_ ^ SGN_BOTH, c0, c1} : u32x4{c0, c1, s0, s1_};
#pragma unroll
    for (int q = 0; q < 2; ++q) {
      const float a = (float)(((c >> 1) * (s1 + 256 * (2 * g + q))) & 2047) * (1.f / 2048), cc = cosr(a), ss = sinr(a);
      G[q] = (c & 1) ? pk(-ss, cc) : pk(cc, ss);
    }
  }
  PIN(Bc); PIN(Bs); PIN(Am); PIN(G); SB();
  if (POLL) { xd = poll4(rx, xo, xs, cap); STAMP(10); }
  char* tr = scr + 16384 + w * 1280;
  *reinterpret_cast<u32x4*>(tr + g * 288 + c * 16) = xd;
  float v[8], s = 0.f;
#pragma unroll
  for (int p = 0; p < 4; ++p) s += blo(xd[p]) + bhi(xd[p]);
  __builtin_amdgcn_fence(__ATOMIC_RELEASE, "wavefront"); __builtin_amdgcn_wave_barrier(); __builtin_amdgcn_fence(__ATOMIC_ACQUIRE, "wavefront");
#pragma unroll
  for (int j = 0; j < 8; ++j) v[j] = blo(*reinterpret_cast<const u16*>(tr + g * 288 + j * 32 + c * 2));
  const float mean = wave_sum(s) * (1.f / 512);
  float q = 0.f;
#pragma unroll
  for (int j = 0; j < 8; ++j) { v[j] -= mean; q += v[j] * v[j]; }
  const float rs = __builtin_amdgcn_rsqf(wave_sum(q) * (1.f / 512) + 1e-5f);
  const u32x4 a = {pk(v[0] * rs, v[1] * rs), pk(v[2] * rs, v[3] * rs), pk(v[4] * rs, v[5] * rs), pk(v[6] * rs, v[7] * rs)};
  const u32 ny = c == 0 ? 0xBF803F80u : 0u;
  u32* L = reinterpret_cast<u32*>(scr);
#pragma unroll
  for (int t = 0; t < 2; ++t) {
    const u32 f = t ? SGN_HI : 0u;
    const f32x4 dr = mma32(a, Bc ^ f, Z4), di = mma32(a, Bs ^ f, Z4);
    f32x4 re, im;
#pragma unroll
    for (int i = 0; i < 4; ++i) { re[i] = dr[i] * tc[t][i] + di[i] * ts[t][i]; im[i] = di[i] * tc[t][i] - dr[i] * ts[t][i]; }
    const u32x4 b = packc(re, im);
    f32x4 R = mma32(Am, b, Z4);
    if (t == 0) { const f32x4 n = mma32(u32x4{ny, ny, 0u, 0u}, b, Z4); if (l == 0) { R[0] = n[0]; R[1] = 0.f; } }
#pragma unroll
    for (int q2 = 0; q2 < 2; ++q2) L[((w >> 1) * 264 + c + 16 * t + 32 * (2 * g + q2)) * 2 + (w & 1)] = pk(R[2 * q2], R[2 * q2 + 1]);
  }
  __syncthreads();
  const f32x4 o0 = mma16(G, reinterpret_cast<const u32x2*>(L)[g * 264 + c + 32 * w], Z4), o1 = mma16(G, reinterpret_cast<const u32x2*>(L)[g * 264 + c + 32 * w + 16], Z4);
  st4<POLL ? 16 : 0>(swp(u32x4{san(pk(o0[0], o0[1])), san(pk(o0[2], o0[3])), san(pk(o1[0], o1[1])), san(pk(o1[2], o1[3]))}), rw,
                     ((c + 32 * w + 16 * (g & 1)) * 256 + s1) * 32 + (g & 2) * 8, zs);
}

__global__ __launch_bounds__(512, 2) void k_pre(const u16* __restrict__ x, const u16* __restrict__ wr, const u16* __restrict__ wi, const u16* __restrict__ wg,
                                                const u16* __restrict__ wu, const u16* __restrict__ wd, char* __restrict__ ws DARG) {
  __shared__ __attribute__((aligned(16))) char scr[16384 + 8 * 1280 + 256];
  u32 stv = 0; (void)stv;
  const int b = blockIdx.x, tid = threadIdx.x;
  if (b >= 256) {
    if (b < 256 + 16) {
      const char *c0, *c1;
      asm volatile("s_getpc_b64 vcc\n\ts_add_u32 vcc_lo, vcc_lo, k_main@rel32@lo+4\n\ts_addc_u32 vcc_hi, vcc_hi, k_main@rel32@hi+12\n\ts_mov_b64 %0, vcc" : "=s"(c0) :: "vcc", "scc");
      asm volatile("s_getpc_b64 vcc\n\ts_add_u32 vcc_lo, vcc_lo, k_end@rel32@lo+4\n\ts_addc_u32 vcc_hi, vcc_hi, k_end@rel32@hi+12\n\ts_mov_b64 %0, vcc" : "=s"(c1) :: "vcc", "scc");
      const long n = c1 - c0, o = (long)tid * 128;
      if (n < 65536 && o < n) { u32 d; asm volatile("global_load_dword %0, %1, off\n\ts_waitcnt vmcnt(0)" : "=v"(d) : "v"(c0 + o) : "memory"); }
    }
    const int it = (b - 256) * 8 + __builtin_amdgcn_readfirstlane(tid >> 6);
    u32 v[3][8]; float sc[3]; u32x4* dst[3];
#pragma unroll
    for (int q = 0; q < 3; ++q) {
      const Item I = pre_item(it + 2080 * q, tid & 63, wr, wi, wg, wu, wd, (u32x4*)(ws + O_WT), (u32x4*)(ws + O_WG), (u32x4*)(ws + O_WD));
      sc[q] = I.sc; dst[q] = I.dst;
#pragma unroll
      for (int j = 0; j < 8; ++j) v[q][j] = I.src[(j & 1) * I.A + ((j >> 1) & 1) * I.B + (j >> 2) * I.C];
    }
#pragma unroll
    for (int q = 0; q < 3; ++q)
      *dst[q] = u32x4{pk(blo(v[q][0]) * sc[q], blo(v[q][1]) * sc[q]), pk(blo(v[q][2]) * sc[q], blo(v[q][3]) * sc[q]),
                      pk(blo(v[q][4]) * sc[q], blo(v[q][5]) * sc[q]), pk(blo(v[q][6]) * sc[q], blo(v[q][7]) * sc[q])};
    return;
  }
  STAMP(0);
  const rsrc_t rw = mkrs(ws, WS_END), rx = mkrs(x, 2 * MB);
  if (tid < 4) *reinterpret_cast<u32*>(ws + O_CNT + (b * 4 + tid) * 256) = 0u;
  int cap = 0;
  phase_a1<false>(rx, 0, rw, O_Z1, scr, (b & 7) * 32 + (b >> 3), __builtin_amdgcn_readfirstlane(tid >> 6), tid & 63, cap SPASS);
  st4<0>(u32x4{SENT, SENT, SENT, SENT}, rw, (b * 512 + tid) * 16, O_Y1);
  STAMP(1); VW(); STAMP(4); FLUSH(19);
}

DEV void glds16(const void* gsrc, u32 lds_dst) {
  u32 keep;
  asm volatile("s_mov_b32 %0, m0\n\ts_mov_b32 m0, %2\n\ts_nop 0\n\tglobal_load_lds_dwordx4 %1, off\n\ts_mov_b32 m0, %0" : "=&s"(keep) : "v"(gsrc), "s"(lds_dst) : "memory");
}
#define XW 10
#define XA 16
#define PRIO 2
constexpr int HW = 5;

extern "C" __global__ __launch_bounds__(512, 2) void k_main(const u16* __restrict__ x, char* __restrict__ ws, u16* __restrict__ out DARG) {
  extern __shared__ __attribute__((aligned(16))) u32x4 SM[];
  char* scr = reinterpret_cast<char*>(SM) + 98304;
  u32 stv = 0; (void)stv;
  const int b = blockIdx.x, tid0 = threadIdx.x, w0 = __builtin_amdgcn_readfirstlane(tid0 >> 6);
  const int grp = b & 7, r_ = b >> 3, s1 = grp * 32 + r_;
  const rsrc_t rw = mkrs(ws, WS_END), rx0 = mkrs(x, 2 * MB), ro = mkrs(out, 2 * MB);
  int cap = SPIN_CAP;
  STAMP(0);
  const u32x4 wt = ld4<0>(rw, tid0 * 16, O_WT + s1 * 8192);
  const int NG = r_ & 7, TG = grp * 4 + (r_ >> 3), F0 = r_ * 3, rot0 = (r_ * 9) & 31, rot20 = (NG * 11) >> 3;
  u32x4 wd[11][2];
#define FSTEP(s) (11 * w + (s) + rot2 - ((s) + rot2 >= 11 ? 11 : 0))
#define LDWD(s) { const u32 o_ = O_WD + (2 * NG * 88 + FSTEP(s)) * 1024; wd[s][0] = ld4<0>(rw, l * 16, o_); wd[s][1] = ld4<0>(rw, l * 16, o_ + 88 * 1024); }
  const u32 lb = (u32)(size_t)(__attribute__((address_space(3))) u32x4*)SM;
#define LDGU(i) { const char* src = ws + O_WG + (min(F0 + (i), NF - 1) * 32 + w * 4) * 1024 + l * 16; \
    _Pragma("unroll") for (int j = 0; j < 4; ++j) glds16(src + j * 1024, lb + (((4 * w + j - rot) & 31) * 3 + (i)) * 1024); }

  for (int L = 0; L < 2; ++L) {
    {
      LANE();
      u32x4 zd;
      if (L == 0) zd = ld4<0>(rw, tid * 16, O_Z1 + s1 * 8192);
      float cs[4], sn[4], tc[4], ts[4];
#pragma unroll
      for (int j = 0; j < 4; ++j) {
        const int p = (4 * g + j) * c;
        const float a = (float)(p & 15) * (1.f / 16), bb = (float)p * (1.f / 256);
        cs[j] = cosr(a); sn[j] = sinr(a); tc[j] = cosr(bb); ts[j] = sinr(bb); PIN(tc[j]); PIN(ts[j]);
      }
      u32x4 B1r = {pk(cs[0], sn[0]), pk(cs[1], sn[1]), pk(cs[2], sn[2]), pk(cs[3], sn[3])};
      u32x4 B1i = {pk(-sn[0], cs[0]), pk(-sn[1], cs[1]), pk(-sn[2], cs[2]), pk(-sn[3], cs[3])};
      u32x4 Fr = {pk(cs[0], cs[1]), pk(cs[2], cs[3]), pk(sn[0], sn[1]), pk(sn[2], sn[3])};
      PIN(B1r); PIN(B1i); PIN(Fr); SB();
      if (L == 1) zd = poll4(rw, tid * 16, O_Z2 + s1 * 8192, cap);
      u32* P = reinterpret_cast<u32*>(scr + 8448);
#pragma unroll
      for (int i = 0; i < 4; ++i) { const int sa = tid >> 1; P[(4 * (tid & 1) + i) * 312 + sa + 16 * (sa >> 6)] = zd[i]; }
      if (L == 0) st4<16>(u32x4{SENT, SENT, SENT, SENT}, rw, tid * 16, O_Z2 + s1 * 8192);
      if (L == 0) LDGU(0)
      asm volatile("s_waitcnt lgkmcnt(0)" ::: "memory"); __builtin_amdgcn_s_barrier(); asm volatile("" ::: "memory");
      STAMP(1 + 12 * L);
      u32x4 a;
#pragma unroll
      for (int j = 0; j < 4; ++j) a[j] = P[w * 312 + 80 * g + 16 * j + c];
      const u32x4 Fi = {Fr[2] ^ SGN_BOTH, Fr[3] ^ SGN_BOTH, Fr[0], Fr[1]}, Ir = {Fr[0], Fr[1], Fi[0], Fi[1]}, Ii = {Fr[2], Fr[3], Fr[0], Fr[1]};
      f32x4 dr = mma32(a, B1r, Z4), di = mma32(a, B1i, Z4), re, im;
#pragma unroll
      for (int i = 0; i < 4; ++i) { re[i] = dr[i] * tc[i] + di[i] * ts[i]; im[i] = di[i] * tc[i] - dr[i] * ts[i]; }
      u32x4 bb = packc(re, im);
      dr = mma32(Fr, bb, Z4); di = mma32(Fi, bb, Z4);
#pragma unroll
      for (int i = 0; i < 4; ++i) { const float wr = blo(wt[i]), wi = bhi(wt[i]); re[i] = dr[i] * wr - di[i] * wi; im[i] = di[i] * wr + dr[i] * wi; }
      bb = packc(re, im);
      dr = mma32(bb, Ir, Z4); di = mma32(bb, Ii, Z4);
#pragma unroll
      for (int i = 0; i < 4; ++i) { re[i] = dr[i] * tc[i] - di[i] * ts[i]; im[i] = di[i] * tc[i] + dr[i] * ts[i]; }
      bb = packc(re, im);
      dr = mma32(Ir, bb, Z4); di = mma32(Ii, bb, Z4);
      u32* Q = reinterpret_cast<u32*>(scr + 20480);
#pragma unroll
      for (int i = 0; i < 4; ++i) Q[(16 * (4 * g + i) + c) * 8 + (w ^ (((c >> 2) & 1) * 4))] = san(pk(dr[i], di[i]));
      asm volatile("s_waitcnt vmcnt(4) lgkmcnt(0)" ::: "memory"); __builtin_amdgcn_s_barrier(); asm volatile("" ::: "memory");
      st4<16>(reinterpret_cast<const u32x4*>(Q)[tid ^ ((tid >> 3) & 1)], rw, ((tid >> 1) * 256 + s1) * 32 + (tid & 1) * 16, L ? O_Y2 : O_Y1);
      STAMP(2 + 12 * L);
    }
    {
      LANE();
      const u32 xo = (s1 + 256 * w) * 1024 + 32 * c + 8 * (g & 2) + 512 * (g & 1);
      u32x4 xr;
      if (L == 0) xr = ld4<0>(rx0, xo, 0); else xr = ld4<16>(rw, xo, O_X2);
      u32x2 G, Br, Bi; u32x4 B2; float tc[2][4], ts[2][4];
#pragma unroll
      for (int q = 0; q < 2; ++q) {
        const float a = (float)(((2 * g + q) * (s1 + 256 * (c >> 1))) & 2047) * (1.f / 2048), cc = cosr(a), ss = sinr(a);
        G[q] = (c & 1) ? pk(ss, cc) : pk(cc, -ss);
        const float bb = (float)(((2 * g + q) * c) & 15) * (1.f / 16), cb = cosr(bb), sb = sinr(bb);
        Br[q] = pk(cb, -sb); Bi[q] = pk(sb, cb);
      }
#pragma unroll
      for (int t = 0; t < 2; ++t)
#pragma unroll
        for (int i = 0; i < 4; ++i) { const float a = (float)((16 * t + 4 * g + i) * c) * (1.f / 512); tc[t][i] = cosr(a); ts[t][i] = sinr(a); PIN(tc[t][i]); PIN(ts[t][i]); }
      {
        float cs[4], sn[4];
#pragma unroll
        for (int j = 0; j < 4; ++j) { const float a = (float)(((4 * g + j) * c) & 31) * (1.f / 32); cs[j] = cosr(a); sn[j] = -sinr(a); }
        B2 = u32x4{pk(cs[0], cs[1]), pk(cs[2], cs[3]), pk(sn[0], sn[1]), pk(sn[2], sn[3])};
      }
      PIN(G); PIN(Br); PIN(Bi); PIN(B2); SB();
      const u32x4 y = swp(poll4(rw, (s1 * 256 + c + 32 * w + 16 * (g & 1)) * 32 + (g & 2) * 8, L ? O_Y2 : O_Y1, cap));
      if (L == 0) {
        st4<16>(u32x4{SENT, SENT, SENT, SENT}, rw, tid * 16, O_Y2 + s1 * 8192);
        st4<16>(u32x4{SENT, SENT, SENT, SENT}, rw, l * 16, O_X2 + (s1 + 256 * w) * 1024);
        LDGU(1) LDWD(0) LDWD(1)
      }
      STAMP(3 + 12 * L);
      u32x2* Lp = reinterpret_cast<u32x2*>(scr);
#pragma unroll
      for (int u = 0; u < 2; ++u) { const f32x4 o = mma16(G, u32x2{y[2 * u], y[2 * u + 1]}, Z4); Lp[g * 264 + c + 16 * (2 * w + u)] = u32x2{pk(o[0], o[1]), pk(o[2], o[3])}; }
      asm volatile("s_waitcnt lgkmcnt(0)" ::: "memory"); __builtin_amdgcn_s_barrier(); asm volatile("" ::: "memory");
      u32 r[2][2];
#pragma unroll
      for (int t = 0; t < 2; ++t)
#pragma unroll
        for (int q = 0; q < 2; ++q) r[t][q] = reinterpret_cast<const u32*>(scr)[((w >> 1) * 264 + c + 16 * t + 32 * (2 * g + q)) * 2 + (w & 1)];
      const float nyq = blo(__builtin_amdgcn_readfirstlane(r[0][0]));
      if (l == 0) r[0][0] = 0u;
      f32x4 acc[2];
      xr = swp(xr);
#pragma unroll
      for (int t = 0; t < 2; ++t) acc[t] = f32x4{blo(xr[2 * t]), bhi(xr[2 * t]), blo(xr[2 * t + 1]), bhi(xr[2 * t + 1])};
#pragma unroll
      for (int t = 0; t < 2; ++t) {
        const u32x2 a = {r[t][0], r[t][1]};
        f32x4 dr = mma16(a, Br, Z4), di = mma16(a, Bi, Z4), re, im;
        if (t == 0) dr[0] += g == 0 ? ((c & 1) ? -nyq : nyq) : 0.f;
#pragma unroll
        for (int i = 0; i < 4; ++i) { re[i] = dr[i] * tc[t][i] - di[i] * ts[t][i]; im[i] = di[i] * tc[t][i] + dr[i] * ts[t][i]; }
        const u32x4 a2 = packc(re, im);
        const u32 f = (t && (c & 1)) ? SGN_BOTH : 0u;
        acc[0] = mma32(a2, B2 ^ f, acc[0]); acc[1] = mma32(a2, B2 ^ (f ^ SGN_HI), acc[1]);
      }
      const u32x4 xq = packc(acc[0], acc[1]);
      st4<16>(swp(xq), rw, xo, O_X1 + L * 2 * MB);
      float v[8], s = 0.f;
#pragma unroll
      for (int p = 0; p < 4; ++p) { v[2 * p] = blo(xq[p]); v[2 * p + 1] = bhi(xq[p]); s += v[2 * p] + v[2 * p + 1]; }
      const float mean = wave_sum(s) * (1.f / 512);
      float q = 0.f;
#pragma unroll
      for (int j = 0; j < 8; ++j) { v[j] -= mean; q += v[j] * v[j]; }
      const float rs = __builtin_amdgcn_rsqf(wave_sum(q) * (1.f / 512) + 1e-5f);
      u32x4* Xt = reinterpret_cast<u32x4*>(scr + 8448);
      Xt[l * 8 + (w ^ (l & 7))] = u32x4{pk(v[0] * rs, v[1] * rs), pk(v[2] * rs, v[3] * rs), pk(v[4] * rs, v[5] * rs), pk(v[6] * rs, v[7] * rs)};
      asm volatile("s_waitcnt lgkmcnt(0)" ::: "memory"); __builtin_amdgcn_s_barrier(); asm volatile("" ::: "memory");
      st4<16>(Xt[tid ^ ((tid >> 3) & 7)], rw, ((s1 >> 2) * 2048 + (tid >> 3) * 32 + (s1 & 3) * 8 + (tid & 7)) * 16, O_XN + L * 2 * MB);
      STAMP(4 + 12 * L);
    }
    gu32* cnt = (gu32*)(ws + O_CNT + (L * 16 + grp) * 8192);
    arrive(cnt + (r_ & (NSH - 1)) * 64); STAMP(24 + 4 * L);
    LANE();
    if (L == 0) { LDGU(2) LDWD(2) LDWD(3) DRAIN(); }
    if (r_ < 29) {
      await(cnt, 31, cap, w, l);
      STAMP(5 + 12 * L);
      const u32 xo = (grp * 8 + w) * 32768 + l * 16, xs = O_XN + L * 2 * MB;
      u32x4 xf[32], wf[2][3];
#pragma unroll
      for (int i = 0; i < XW; ++i) xf[i] = ld4<XA>(rw, xo, xs + ((i + rot) & 31) * 1024);
#pragma unroll
      for (int i = 0; i < 3; ++i) wf[0][i] = SM[i * 64 + l];
      f32x16 acc[3] = {};
      if (w < 4) __builtin_amdgcn_s_setprio(PRIO);
      SB();
#pragma unroll
      for (int ks = 0; ks < 32; ++ks) {

        if (ks + XW < 32) xf[ks + XW] = ld4<XA>(rw, xo, xs + ((ks + XW + rot) & 31) * 1024);
        if (ks + 1 < 32) {
#pragma unroll
          for (int i = 0; i < 3; ++i) wf[(ks + 1) & 1][i] = SM[((ks + 1) * 3 + i) * 64 + l];
        }
        SB();
#pragma unroll
        for (int i = 0; i < 3; ++i) acc[i] = mmb(wf[ks & 1][i], xf[ks], acc[i]);
        SB();
      }
#pragma unroll
      for (int i = 0; i < 3; ++i) {
        float h[8];
#pragma unroll
        for (int r = 0; r < 8; ++r) h[r] = fmaxf(acc[i][r], 0.f) * acc[i][r + 8];
        if (F0 + i < NF) st4<16>(u32x4{pk(h[0], h[1]), pk(h[2], h[3]), pk(h[4], h[5]), pk(h[6], h[7])}, rw, ((grp * 8 + w) * NF + F0 + i) * 1024 + l * 16, O_H + L * 6 * MB);
      }
      __builtin_amdgcn_s_setprio(0);
      STAMP(6 + 12 * L);
      arrive(cnt + 8 * 2048 + (r_ & (NSH - 1)) * 64); STAMP(25 + 4 * L);
    }
    if (L == 0) { LDWD(4) LDWD(5) LDWD(6) LDWD(7) LDWD(8) LDWD(9) LDWD(10) }
    {
      await(cnt + 8 * 2048, 28, cap, w, l);
      STAMP(7 + 12 * L);
      const u32 hs = O_H + L * 6 * MB + 2 * TG * NF * 1024;
      const u32 oo = (8 * TG + w + 256 * (l >> 3)) * 1024 + NG * 128 + (l & 7) * 16;
      u32x4 hf[11][2];
#define LDH(s) { const u32 o_ = hs + min(FSTEP(s), NF - 1) * 1024; hf[s][0] = ld4<16>(rw, l * 16, o_); hf[s][1] = ld4<16>(rw, l * 16, o_ + NF * 1024); }
#pragma unroll
      for (int s = 0; s < HW; ++s) LDH(s)
      f32x16 acc[4] = {};
      u32x4 res;
      SB();
#pragma unroll
      for (int s = 0; s < 11; ++s) {
        if (s + HW < 11) LDH(s + HW)
        if (s + HW == 11) res = ld4<16>(rw, oo, O_X1 + L * 2 * MB);
        SB();
#pragma unroll
        for (int q = 0; q < 4; ++q) acc[q] = mmb(wd[s][q & 1], hf[s][q >> 1], acc[q]);
        SB();
      }
      STAMP(8 + 12 * L);
      u32x4* Rd = reinterpret_cast<u32x4*>(scr);
#pragma unroll
      for (int d = 0; d < 8; ++d) {
        const f32x16 A = acc[d >> 1]; const int o = 8 * (d & 1);
        Rd[w * 512 + (32 * (d >> 2) + (l & 31)) * 8 + ((4 * ((d >> 1) & 1) + 2 * (l >> 5) + (d & 1)) ^ (l & 7))] =
            u32x4{pk(A[o], A[o + 1]), pk(A[o + 2], A[o + 3]), pk(A[o + 4], A[o + 5]), pk(A[o + 6], A[o + 7])};
      }
      asm volatile("s_waitcnt lgkmcnt(0)" ::: "memory"); __builtin_amdgcn_s_barrier(); asm volatile("" ::: "memory");
      STAMP(26 + 4 * L);
      float own[8] = {};
#pragma unroll
      for (int j = 0; j < 8; ++j) {
        const u32x4 p = Rd[j * 512 + w * 64 + (l ^ (l >> 3))];
#pragma unroll
        for (int i = 0; i < 4; ++i) { own[2 * i] += blo(p[i]); own[2 * i + 1] += bhi(p[i]); }
      }
      u32x4 o;
#pragma unroll
      for (int i = 0; i < 4; ++i) o[i] = pk(own[2 * i] + blo(res[i]), own[2 * i + 1] + bhi(res[i]));
      if (L == 1) { st4<0>(o, ro, oo, 0); STAMP(21); VW(); STAMP(22); FLUSH(r_ < 29 ? 0x77FFEFFFu : 0x557FEFFFu & ~0x60060u); break; }
      st4<16>(u32x4{san(o[0]), san(o[1]), san(o[2]), san(o[3])}, rw, oo, O_X2);
      asm volatile("s_waitcnt lgkmcnt(0)" ::: "memory"); __builtin_amdgcn_s_barrier(); asm volatile("" ::: "memory");
      STAMP(9);
    }
    { LANE(); phase_a1<true>(rw, O_X2, rw, O_Z2, scr, s1, w, l, cap SPASS); STAMP(11); }
  }
}

extern "C" __global__ void k_end() {}

extern "C" void kernel_launch(void* const* d_in, const int* in_sizes, int n_in, void* d_out, int out_size,
                              void* d_ws, size_t ws_size, hipStream_t stream) {
  (void)hipFuncSetAttribute((const void*)k_main, hipFuncAttributeMaxDynamicSharedMemorySize, 163840);
  k_pre<<<256 + 260, 512, 0, stream>>>((const u16*)d_in[0], (const u16*)d_in[1], (const u16*)d_in[2], (const u16*)d_in[4], (const u16*)d_in[5],
                                       (const u16*)d_in[6], (char*)d_ws DPASS(0));
  k_main<<<256, 512, 163840, stream>>>((const u16*)d_in[0], (char*)d_ws, (u16*)d_out DPASS(1));
}
```
